```python
import torch

def _softmax(x, axis=-1):
    return torch.softmax(x, dim=axis)
def _gelu(x):
    return torch.nn.functional.gelu(x, approximate="tanh")
def _silu(x):
    return torch.nn.functional.silu(x)
def _sigmoid(x):
    return torch.sigmoid(x)
def _layernorm(x, axis=-1, eps=1e-5):
    m = x.mean(dim=axis, keepdim=True)
    v = x.var(dim=axis, keepdim=True, unbiased=False)
    return (x - m) / torch.sqrt(v + eps)
def _scatter_add(tgt, idx, src, axis=0):
    return tgt.clone().index_add_(axis, idx, src)
def _segment_sum(src, seg, n):
    out = torch.zeros((n,) + src.shape[1:], dtype=src.dtype)
    return out.index_add_(0, seg, src)
def _pad(x, pw):
    flat = tuple(v for pair in reversed(pw) for v in pair)
    return torch.nn.functional.pad(x, flat)

kernel_name = 'parallel_sum__sliding_block__moe_arch_mod__prompt'

def setup_inputs():

    _w = (lambda a: (lambda t: t.to(torch.bfloat16) if t.is_floating_point() else t)(torch.as_tensor(a)))
    def _A_setup():
        B, S, D = 1, 2048, 512
        return {
            "x": _w((lambda *s: torch.randn(*s, dtype=torch.bfloat16) * 0.05)(B, S, D)),
            "wqkv": _w((lambda *s: torch.randn(*s, dtype=torch.bfloat16) * 0.05)(D, 3 * D)),
            "wo": _w((lambda *s: torch.randn(*s, dtype=torch.bfloat16) * 0.05)(D, D)),
        }
    _A_in = _A_setup()

    def _B_setup():
        B, S, D, F, C = 1, 2048, 512, 1376, 1
        return {
            "x": _w((lambda *s: torch.randn(*s, dtype=torch.bfloat16) * 0.05)(B, S, D)),
            "w_r": _w((lambda *s: torch.randn(*s, dtype=torch.bfloat16) * 0.05)(D)),
            "w_gate": _w((lambda *s: torch.randn(*s, dtype=torch.bfloat16) * 0.05)(D, F)),
            "w_up": _w((lambda *s: torch.randn(*s, dtype=torch.bfloat16) * 0.05)(D, F)),
            "w_down": _w((lambda *s: torch.randn(*s, dtype=torch.bfloat16) * 0.05)(F, D)),
        }
    _B_in = _B_setup()

    return {
        "A_x": _A_in["x"],
        "A_wqkv": _A_in["wqkv"],
        "A_wo": _A_in["wo"],
        "B_x": _B_in["x"],
        "B_w_r": _B_in["w_r"],
        "B_w_gate": _B_in["w_gate"],
        "B_w_up": _B_in["w_up"],
        "B_w_down": _B_in["w_down"],
    }

def reference(A_x, A_wqkv, A_wo, B_x, B_w_r, B_w_gate, B_w_up, B_w_down):
    def _A_ref(x, wqkv, wo):
        B, S, D = x.shape
        H = 32; Dh = D // H; W = 256
        qkv = (lambda x, *d: x.permute(*d))((x @ wqkv).reshape(B, S, 3, H, Dh), 2, 0, 3, 1, 4)
        q, k, v = qkv[0], qkv[1], qkv[2]
        scores = torch.einsum("bhqd,bhkd->bhqk", q, k) * (Dh ** -0.5)
        idx = torch.arange(S)
        mask = (idx[:, None] - idx[None, :] >= 0) & (idx[:, None] - idx[None, :] < W)
        scores = torch.where(mask[None, None], scores, -1e9)
        p = _softmax(scores, axis=-1)
        o = torch.einsum("bhqk,bhkd->bhqd", p, v)
        return (lambda x, *d: x.permute(*d))(o, 0, 2, 1, 3).reshape(B, S, D) @ wo

    def _B_ref(x, w_r, w_gate, w_up, w_down):
        B, S, D = x.shape
        C = 1
        r = x @ w_r
        idx = torch.argsort(-r, axis=-1)[:, :C]
        idx_e = (lambda x, n, axis: torch.repeat_interleave(x, n, dim=axis))(idx.reshape(B, C, 1), D, axis=2)
        x_sel = (lambda x, i, axis: torch.take_along_dim(x, i, dim=axis))(x, idx_e, axis=1)
        g = _silu(x_sel @ w_gate)
        u = x_sel @ w_up
        y = (g * u) @ w_down
        r_sel = _sigmoid((lambda x, i, axis: torch.take_along_dim(x, i, dim=axis))(r, idx, axis=1))
        y = y * r_sel.reshape(B, C, 1)
        b_off = (torch.arange(B) * S).reshape(B, 1)
        flat_idx = (idx + b_off).reshape(B * C)
        zero = (x * 0.0).reshape(B * S, D)
        upd = _scatter_add(zero, flat_idx, y.reshape(B * C, D), axis=0)
        return x + upd.reshape(B, S, D)

    x = A_x
    xn = _layernorm(x, axis=-1)
    return x + _A_ref(xn, A_wqkv, A_wo) + _B_ref(xn, B_w_r, B_w_gate, B_w_up, B_w_down)
```

```cpp
#include <hip/hip_runtime.h>
#include <hip/hip_bf16.h>
typedef unsigned short u16;
__device__ __forceinline__ float bf2f(u16 v){ return __uint_as_float(((unsigned)v)<<16); }
__device__ __forceinline__ u16 f2bf(float f){ unsigned u=__float_as_uint(f); u = (u + 0x7FFF + ((u>>16)&1))>>16; return (u16)u; }
__device__ __forceinline__ float rbf(float f){ return bf2f(f2bf(f)); }
constexpr int S=2048, D=512, H=32, DH=16, W=256, F=1376;

__global__ void k_ln(const u16* x, const u16* wr, u16* xn, float* r){
  int row=blockIdx.x; int t=threadIdx.x;
  float v[8]; float s=0;
  for(int i=0;i<8;i++){ v[i]=bf2f(x[row*D+t*8+i]); s+=v[i]; }
  for(int m=32;m>=1;m>>=1) s+=__shfl_xor(s,m);
  float mean=s/D; float q=0;
  for(int i=0;i<8;i++){ float d=v[i]-mean; q+=d*d; }
  for(int m=32;m>=1;m>>=1) q+=__shfl_xor(q,m);
  float var=rbf(q/D); float mb=rbf(mean);
  float sd=rbf(sqrtf(rbf(var+1e-5f)));
  float rr=0;
  for(int i=0;i<8;i++){ float y=rbf(rbf(v[i]-mb)/sd); xn[row*D+t*8+i]=f2bf(y); rr+=y*bf2f(wr[t*8+i]); }
  for(int m=32;m>=1;m>>=1) rr+=__shfl_xor(rr,m);
  if(t==0) r[row]=rbf(rr);
}
__global__ void k_gemm(const u16* A, const u16* B, u16* C, int M,int N,int K){
  int n=blockIdx.x*blockDim.x+threadIdx.x; int m=blockIdx.y;
  float acc=0; for(int k=0;k<K;k++) acc+=bf2f(A[m*K+k])*bf2f(B[k*N+n]);
  C[m*N+n]=f2bf(acc);
}
__global__ void k_attn(const u16* qkv, u16* o){
  int q=blockIdx.x*blockDim.x+threadIdx.x; int h=blockIdx.y;
  float qv[16]; for(int d=0;d<16;d++) qv[d]=bf2f(qkv[q*1536+h*16+d]);
  int k0=q-255; if(k0<0)k0=0;
  float mx=-1e30f;
  for(int k=k0;k<=q;k++){ float s=0; for(int d=0;d<16;d++) s+=qv[d]*bf2f(qkv[k*1536+512+h*16+d]); s*=0.25f; mx=fmaxf(mx,s);}
  float l=0; float acc[16]; for(int d=0;d<16;d++)acc[d]=0;
  for(int k=k0;k<=q;k++){ float s=0; for(int d=0;d<16;d++) s+=qv[d]*bf2f(qkv[k*1536+512+h*16+d]); s*=0.25f; float p=__expf(s-mx); l+=p; for(int d=0;d<16;d++) acc[d]+=p*bf2f(qkv[k*1536+1024+h*16+d]);}
  for(int d=0;d<16;d++) o[q*512+h*16+d]=f2bf(acc[d]/l);
}
__global__ void k_moe1(const float* r, const u16* xn, const u16* wg, const u16* wu, float* h, int* idxp){
  __shared__ int sidx;
  if(threadIdx.x==0){ int b=0; float bv=r[0]; for(int i=1;i<S;i++) if(r[i]>bv){bv=r[i];b=i;} sidx=b; if(blockIdx.x==0)*idxp=b; }
  __syncthreads();
  int f=blockIdx.x*blockDim.x+threadIdx.x; if(f>=F) return;
  float g=0,u=0; for(int k=0;k<D;k++){ float xv=bf2f(xn[sidx*D+k]); g+=xv*bf2f(wg[k*F+f]); u+=xv*bf2f(wu[k*F+f]); }
  g=rbf(g); u=rbf(u); g=rbf(g/(1.f+__expf(-g))); h[f]=rbf(g*u);
}
__global__ void k_moe2(const float* r,const float* h,const u16* wd,const int* idxp,float* y){
  int n=blockIdx.x*blockDim.x+threadIdx.x; float a=0; for(int f=0;f<F;f++) a+=h[f]*bf2f(wd[f*D+n]);
  float rs=rbf(1.f/(1.f+__expf(-r[*idxp]))); y[n]=rbf(rbf(a)*rs);
}
__global__ void k_fin(const u16* x,const u16* xn,const u16* a,const float* y,const int* idxp,u16* out){
  int i=blockIdx.x*blockDim.x+threadIdx.x; int row=i/D, c=i%D;
  float b=bf2f(xn[i]); if(row==*idxp) b=rbf(b+y[c]);
  out[i]=f2bf(rbf(bf2f(x[i])+bf2f(a[i]))+b);
}
extern "C" void kernel_launch(void* const* d_in, const int* in_sizes, int n_in, void* d_out, int out_size, void* d_ws, size_t ws_size, hipStream_t stream){
  const u16* x=(const u16*)d_in[0]; const u16* wqkv=(const u16*)d_in[1]; const u16* wo=(const u16*)d_in[2];
  const u16* wr=(const u16*)d_in[4]; const u16* wg=(const u16*)d_in[5]; const u16* wu=(const u16*)d_in[6]; const u16* wd=(const u16*)d_in[7];
  char* ws=(char*)d_ws;
  u16* xn=(u16*)ws; u16* qkv=(u16*)(ws+(4<<20)); u16* o=(u16*)(ws+(12<<20)); u16* a=(u16*)(ws+(16<<20));
  float* r=(float*)(ws+(20<<20)); float* h=(float*)(ws+(21<<20)); float* y=(float*)(ws+(22<<20)); int* idx=(int*)(ws+(23<<20));
  k_ln<<<S,64,0,stream>>>(x,wr,xn,r);
  k_gemm<<<dim3(1536/256,S),256,0,stream>>>(xn,wqkv,qkv,S,1536,512);
  k_attn<<<dim3(S/64,H),64,0,stream>>>(qkv,o);
  k_gemm<<<dim3(512/256,S),256,0,stream>>>(o,wo,a,S,512,512);
  k_moe1<<<(F+63)/64,64,0,stream>>>(r,xn,wg,wu,h,idx);
  k_moe2<<<D/64,64,0,stream>>>(r,h,wd,idx,y);
  k_fin<<<S*D/256,256,0,stream>>>(x,xn,a,y,idx,(u16*)d_out);
}
```

```cpp
#include <hip/hip_runtime.h>
#include <hip/hip_bf16.h>
#include <stdint.h>

typedef unsigned short u16;
typedef unsigned long long u64;
typedef short bf16x8 __attribute__((ext_vector_type(8)));
typedef short s16x4 __attribute__((ext_vector_type(4)));
typedef float f32x16 __attribute__((ext_vector_type(16)));
typedef float f32x4 __attribute__((ext_vector_type(4)));
typedef float f32x2 __attribute__((ext_vector_type(2)));
typedef unsigned u32x4 __attribute__((ext_vector_type(4)));
typedef unsigned u32x2 __attribute__((ext_vector_type(2)));
typedef int i32x4 __attribute__((ext_vector_type(4)));
typedef __bf16 bf2_t __attribute__((ext_vector_type(2)));
#define DI __device__ __forceinline__
#define LAS __attribute__((address_space(3)))
#define GAS __attribute__((address_space(1)))
typedef GAS unsigned gu32;
typedef GAS u64 gu64;
#define RLX_AGENT __ATOMIC_RELAXED, __HIP_MEMORY_SCOPE_AGENT
#define MFMA32(a, b, c) __builtin_amdgcn_mfma_f32_32x32x16_bf16((a), (b), (c), 0, 0, 0)
#define MFMA16(a, b, c) __builtin_amdgcn_mfma_f32_16x16x32_bf16((a), (b), (c), 0, 0, 0)
#define SBAR() __builtin_amdgcn_sched_barrier(0)
#define CFENCE() asm volatile("" ::: "memory")
#define PIN(x) asm volatile("" : "+v"(x))

#define DIAG 0

constexpr int S_ = 2048, D_ = 512, F_ = 1376;
constexpr unsigned WS_EPOCH = 0;
constexpr unsigned WS_RG = 4096;
constexpr unsigned WS_G1 = 16384;
constexpr unsigned WS_G2 = 393216;
constexpr unsigned WS_FK = 655360;
constexpr unsigned WS_FV = WS_FK + 131072;
constexpr unsigned WS_FO = WS_FV + 131072;
static_assert(WS_FO + 131072 <= (1u << 20), "ws map");
constexpr unsigned WS_K = 1u << 20;
constexpr unsigned WS_V = 3u << 20;
constexpr unsigned WS_O = 5u << 20;
constexpr unsigned WS_DIAG = 7u << 20;
constexpr unsigned WS_USED = 8u << 20;
constexpr unsigned SLOT = 16384;
constexpr unsigned L_WO = 0;
constexpr unsigned L_VREM = 65536, VREM_STRIDE = 8704;
constexpr unsigned L_ONES8 = L_VREM + 4 * VREM_STRIDE;
constexpr unsigned L_KOWN = 131072, L_QOWN = L_KOWN + 8192, L_VOWN = L_QOWN + 8192;
constexpr unsigned L_MISC = L_VOWN + 8192;
constexpr unsigned L_ONES = L_MISC;
constexpr unsigned L_WR = L_MISC + 2048 + 64;
constexpr unsigned L_WORDS = L_MISC + 3200;
constexpr unsigned L_CSP = L_WORDS + 128;
constexpr unsigned L_MEAN = L_CSP + 1536, L_RSTD = L_MEAN + 256;
constexpr unsigned L_Y = L_RSTD + 256;
constexpr unsigned L_LAND = L_Y + 512;
constexpr unsigned L_END = L_LAND + 320;
static_assert(L_END <= 163840, "LDS");
constexpr unsigned W_FREE = L_WORDS, W_PAIR = L_WORDS + 16, W_TAG = L_WORDS + 32, W_VRDY = L_WORDS + 40, W_WORDY = L_WORDS + 48, W_KEY = L_WORDS + 56  ,
                   W_ONES = L_WORDS + 64;
constexpr unsigned SPIN_LDS = 1u << 20, SPIN_GLB = 1u << 16;

DI unsigned rfl(unsigned v) { return __builtin_amdgcn_readfirstlane(v); }
DI unsigned pk2(float a, float b) { f32x2 v = {a, b}; return __builtin_bit_cast(unsigned, __builtin_convertvector(v, bf2_t)); }
DI float rbf(float a) { return __uint_as_float(pk2(a, a) << 16); }
DI float blo(unsigned v) { return __uint_as_float(v << 16); }
DI float bhi(unsigned v) { return __uint_as_float(v & 0xffff0000u); }
DI float dot2(unsigned a, unsigned b, float c) { return __builtin_amdgcn_fdot2_f32_bf16(__builtin_bit_cast(bf2_t, a), __builtin_bit_cast(bf2_t, b), c, false); }
DI float swap32add(float v) { auto r = __builtin_amdgcn_permlane32_swap(__float_as_uint(v), __float_as_uint(v), false, false); return __uint_as_float(r[0]) + __uint_as_float(r[1]); }
#define DPPU(v, ctrl) ((unsigned)__builtin_amdgcn_update_dpp((int)(v), (int)(v), (ctrl), 0xf, 0xf, false))
#define DPPF(v, ctrl) __uint_as_float(DPPU(__float_as_uint(v), ctrl))
DI unsigned wave_max(unsigned k) {
    k = max(k, DPPU(k, 0xB1)); k = max(k, DPPU(k, 0x4E)); k = max(k, DPPU(k, 0x141)); k = max(k, DPPU(k, 0x140));
    return max(max((unsigned)__builtin_amdgcn_readlane((int)k, 0), (unsigned)__builtin_amdgcn_readlane((int)k, 16)),
               max((unsigned)__builtin_amdgcn_readlane((int)k, 32), (unsigned)__builtin_amdgcn_readlane((int)k, 48)));
}
DI float wave_sum(float v) {
    v += DPPF(v, 0xB1); v += DPPF(v, 0x4E); v += DPPF(v, 0x141); v += DPPF(v, 0x140);
    const int u = __float_as_int(v);
    return (__int_as_float(__builtin_amdgcn_readlane(u, 0)) + __int_as_float(__builtin_amdgcn_readlane(u, 16))) +
           (__int_as_float(__builtin_amdgcn_readlane(u, 32)) + __int_as_float(__builtin_amdgcn_readlane(u, 48)));
}
DI float swap16add(float v) { auto r = __builtin_amdgcn_permlane16_swap(__float_as_uint(v), __float_as_uint(v), false, false); return __uint_as_float(r[0]) + __uint_as_float(r[1]); }
template <class T> DI T ldsr(unsigned a) { return *(const LAS T*)(size_t)a; }
template <class T> DI void ldsw(unsigned a, T v) { *(LAS T*)(size_t)a = v; }
DI s16x4 ldtr(unsigned a) { typedef short v4s __attribute__((ext_vector_type(4))); return __builtin_amdgcn_ds_read_tr16_b64_v4i16((LAS v4s*)(size_t)a); }
DI bf16x8 ldtr2(unsigned a0, unsigned a1) { s16x4 lo = ldtr(a0), hi = ldtr(a1); return __builtin_shufflevector(lo, hi, 0, 1, 2, 3, 4, 5, 6, 7); }
DI void lds_flag(unsigned a, unsigned v) { asm volatile("s_mov_b64 exec, 1\n\tds_write_b32 %0, %1\n\ts_mov_b64 exec, -1" ::"v"(a), "v"(v) : "memory"); }
template <int NEED, int SLP = 0> DI void lds_chk(unsigned v, unsigned a) {
    unsigned c;
    asm volatile("v_cmp_gt_u32 vcc, %3, %0\n\ts_cbranch_vccz 2f\n\ts_mov_b32 %1, %4\n"
                 "1:\n\ts_sleep %5\n\tds_read_b32 %0, %2\n\ts_waitcnt lgkmcnt(0)\n\tv_cmp_gt_u32 vcc, %3, %0\n\ts_cbranch_vccz 2f\n\t"
                 "s_sub_u32 %1, %1, 1\n\ts_cbranch_scc0 1b\n2:"
                 : "+v"(v), "=&s"(c) : "v"(a), "i"(NEED), "i"(SPIN_LDS), "i"(SLP) : "vcc", "scc", "memory");
}
template <int NEED, int SLP = 0> DI void lds_wait(unsigned a) {
    unsigned v, c;
    asm volatile("s_mov_b32 %1, %4\n"
                 "1:\n\tds_read_b32 %0, %2\n\ts_waitcnt lgkmcnt(0)\n\tv_cmp_gt_u32 vcc, %3, %0\n\ts_cbranch_vccz 2f\n\ts_sleep %5\n\t"
                 "s_sub_u32 %1, %1, 1\n\ts_cbranch_scc0 1b\n2:"
                 : "=&v"(v), "=&s"(c) : "v"(a), "i"(NEED), "i"(SPIN_LDS), "i"(SLP) : "vcc", "scc", "memory");
}
template <int N, int SLP = 0> DI void land_wait(unsigned a) {
    unsigned v, c;
    asm volatile("s_mov_b32 %1, %4\n"
                 "1:\n\tds_read_b32 %0, %2 offset:%3\n\ts_waitcnt lgkmcnt(0)\n\tv_cmp_ne_u32 vcc, 0, %0\n\ts_cbranch_vccz 2f\n\t"
                 ".if %5\n\ts_sleep %5\n\t.endif\n\ts_sub_u32 %1, %1, 1\n\ts_cbranch_scc0 1b\n2:"
                 : "=&v"(v), "=&s"(c) : "v"(a), "i"(60 - 4 * N), "i"(SPIN_LDS), "i"(SLP) : "vcc", "scc", "memory");
}
template <int N> DI void land_chk(unsigned v, unsigned a) {
    unsigned c;
    asm volatile("v_cmp_ne_u32 vcc, 0, %0\n\ts_cbranch_vccz 2f\n\ts_mov_b32 %1, %4\n"
                 "1:\n\tds_read_b32 %0, %2 offset:%3\n\ts_waitcnt lgkmcnt(0)\n\tv_cmp_ne_u32 vcc, 0, %0\n\ts_cbranch_vccz 2f\n\t"
                 "s_sub_u32 %1, %1, 1\n\ts_cbranch_scc0 1b\n2:"
                 : "+v"(v), "=&s"(c) : "v"(a), "i"(60 - 4 * N), "i"(SPIN_LDS) : "vcc", "scc", "memory");
}
template <int OFF> DI void val_chk(float& d, unsigned a) {
    unsigned c;
    asm volatile("v_cmp_eq_u32 vcc, -1, %0\n\ts_cbranch_vccz 2f\n\ts_mov_b32 %1, %4\n"
                 "1:\n\tds_read_b32 %0, %2 offset:%3\n\ts_waitcnt lgkmcnt(0)\n\tv_cmp_eq_u32 vcc, -1, %0\n\ts_cbranch_vccz 2f\n\t"
                 "s_sub_u32 %1, %1, 1\n\ts_cbranch_scc0 1b\n2:"
                 : "+v"(d), "=&s"(c) : "v"(a), "i"(OFF), "i"(SPIN_LDS) : "vcc", "scc", "memory");
}
DI void bar_lds() { asm volatile("s_waitcnt lgkmcnt(0)\n\ts_barrier" ::: "memory"); }
template <class T> DI T ldsv(unsigned a) { return *(volatile LAS T*)(size_t)a; }
template <int SLP> DI unsigned lds_get(unsigned a) { lds_wait<1, SLP>(a); return rfl(ldsv<unsigned>(a + 4)); }
DI void lds_put(unsigned a, unsigned v) { lds_flag(a + 4, v); lds_flag(a, 1); }
DI void poll_flags(const char* ws, unsigned off, unsigned tag) {
    unsigned v, c;
    asm volatile("s_mov_b32 %1, %5\n"
                 "1:\n\tglobal_load_dword %0, %2, %3 sc1\n\ts_waitcnt vmcnt(0)\n\tv_cmp_ne_u32 vcc, %4, %0\n\ts_cbranch_vccz 2f\n\ts_sleep 1\n\t"
                 "s_sub_u32 %1, %1, 1\n\ts_cbranch_scc0 1b\n2:"
                 : "=&v"(v), "=&s"(c) : "v"(off), "s"(ws), "s"(tag), "i"(SPIN_GLB) : "vcc", "scc", "memory");
}
DI void drain() { asm volatile("s_waitcnt vmcnt(0)" ::: "memory"); }
DI void put_flag(char* ws, unsigned off, unsigned tag) { __hip_atomic_store((gu32*)(ws + off), tag, RLX_AGENT); }
DI void put_gran(char* ws, unsigned off, unsigned tag, unsigned val) { __hip_atomic_store((gu64*)(ws + off), ((u64)tag << 32) | val, RLX_AGENT); }
template <int N, int SLP = 2> DI void sweep(const char* ws, unsigned off, unsigned stride, unsigned tag, unsigned (&v)[N]) {
    for (unsigned s = 0; s < SPIN_GLB; ++s) {
        bool ok = true;
#pragma unroll
        for (int k = 0; k < N; ++k) { u64 x = __hip_atomic_load((gu64*)(ws + off + k * stride), RLX_AGENT); v[k] = (unsigned)x; ok &= (unsigned)(x >> 32) == tag; }
        if (__all(ok)) break;
        __builtin_amdgcn_s_sleep(SLP);
    }
}
DI i32x4 mk_srd(const void* p, unsigned bytes) { u64 a = (u64)p; return (i32x4){(int)(unsigned)a, (int)((unsigned)(a >> 32) & 0xffffu), (int)bytes, 0x00020000}; }
DI void dma(i32x4 srd, unsigned voff, unsigned soff, unsigned ldsdst) {
    asm volatile("s_mov_b32 m0, %3\n\ts_nop 0\n\tbuffer_load_dwordx4 %0, %1, %2 offen lds" ::"v"(voff), "s"(srd), "s"(soff), "s"(ldsdst) : "memory");
}
DI void dma4(i32x4 srd, const unsigned (&v)[4], unsigned soff, unsigned ldsdst) {
    asm volatile("s_mov_b32 m0, %6\n\ts_nop 0\n\tbuffer_load_dwordx4 %0, %4, %5 offen lds\n\tbuffer_load_dwordx4 %1, %4, %5 offen offset:1024 lds\n\t"
                 "buffer_load_dwordx4 %2, %4, %5 offen offset:2048 lds\n\tbuffer_load_dwordx4 %3, %4, %5 offen offset:3072 lds"
                 ::"v"(v[0]), "v"(v[1]), "v"(v[2]), "v"(v[3]), "s"(srd), "s"(soff), "s"(ldsdst) : "memory");
}
DI void dma_zero(i32x4 srd0, unsigned ldsdst) {
    asm volatile("s_mov_b32 m0, %1\n\ts_nop 0\n\tbuffer_load_dword off, %0, 0 lds" ::"s"(srd0), "s"(ldsdst) : "memory");
}
template <int N> DI void wait_vm() { asm volatile("s_waitcnt vmcnt(%0)" ::"i"(N) : "memory"); }
DI bf16x8 asfrag(u32x4 v) { return __builtin_bit_cast(bf16x8, v); }
DI u32x4 asu4(bf16x8 v) { return __builtin_bit_cast(u32x4, v); }
DI u32x4 pack8(const f32x16& a, int o) { return (u32x4){pk2(a[o], a[o + 1]), pk2(a[o + 2], a[o + 3]), pk2(a[o + 4], a[o + 5]), pk2(a[o + 6], a[o + 7])}; }
#define BLOAD(rs, vo, so, aux) __builtin_bit_cast(u32x4, __builtin_amdgcn_raw_buffer_load_b128((rs), (vo), (so), (aux)))
#define BSTORE(v, rs, vo, so, aux) __builtin_amdgcn_raw_buffer_store_b128((v), (rs), (vo), (so), (aux))
constexpr int SC1 = 16;

#define STAMP(i) do { } while (0)
#define CSTAMP(i) do { } while (0)
#define ASTAMP(k) do { } while (0)
#define AFLUSH(n, base) do { } while (0)
#define FSTAMP(i) do { } while (0)

constexpr int NPOS = 16;
constexpr int P_KIND[NPOS] = {0, 0, 1, 0, 1, 0, 1, 1, 2, 2, 2, 2, 3, 3, 3, 3};
constexpr int P_CHUNK[NPOS] = {0, 1, 0, 2, 1, 3, 2, 3, 0, 1, 2, 3, 0, 1, 2, 3};
constexpr int P_SLOT[NPOS] = {0, 1, 4, 2, 5, 3, 6, 7, 0, 4, 1, 5, 2, 6, 3, 7};

__global__ __launch_bounds__(512, 2) void fused_block(const u16* __restrict__ x, const u16* __restrict__ wqkv, const u16* __restrict__ wo,
                                                      const u16* __restrict__ wr, const u16* __restrict__ wg, const u16* __restrict__ wu,
                                                      const u16* __restrict__ wd, u16* __restrict__ out, char* __restrict__ ws) {
    const int tid = threadIdx.x, lane = tid & 63;
    const int wid = rfl(tid >> 6);
    const int b = blockIdx.x, xcd = b & 7, loc = b >> 3;
    const int p = (xcd >> 1) * 8 + (loc >> 2), j = (xcd & 1) * 4 + (loc & 3);
    const int blk = p * 8 + j, rot = (p + j) & 3;
    const int l31 = lane & 31, h = lane >> 5;
    const auto rs_ws = __builtin_amdgcn_make_buffer_rsrc(ws, 0, WS_USED, 0x00020000);
    const auto rs_x = __builtin_amdgcn_make_buffer_rsrc((void*)x, 0, S_ * D_ * 2, 0x00020000);
    u32x4 xchunk, xnorm;
    int stamps = 0; u64 ts_[10];
    STAMP(0); CSTAMP(48);
#define EPI_PREP() do { xchunk = BLOAD(rs_x, (tid >> 3) * 1024 + (tid & 7) * 16, p * 65536 + j * 128, 0); \
        const float mb_ = ldsr<float>(L_MEAN + (tid >> 3) * 4), rs_ = ldsr<float>(L_RSTD + (tid >> 3) * 4); \
        _Pragma("unroll") for (int d = 0; d < 4; ++d) { const unsigned d2 = pk2(blo(xchunk[d]) - mb_, bhi(xchunk[d]) - mb_); xnorm[d] = pk2(blo(d2) * rs_, bhi(d2) * rs_); } \
        PIN(xnorm); } while (0)

    if (wid >= 4) {
        const int lw = wid - 4, r = lane >> 3, ph = lane & 7;
        const unsigned swz = (ph ^ (((r >> 1) & 1) << 2)) * 16;
        const unsigned a_freeall = W_FREE + 4 * (lane & 3);
        unsigned tag;
        if (lw == 0) {
            const i32x4 sx = mk_srd(x, S_ * D_ * 2), sw = mk_srd(wqkv, D_ * 3 * D_ * 2);
            const unsigned vx0 = r * 1024 + ((ph ^ (r >> 1)) * 16), vw0 = r * 3072 + swz;
            const unsigned vx[4] = {vx0, (vx0 ^ 64) + 7168, vx0 + 2 * 7168, (vx0 ^ 64) + 3 * 7168};
            const unsigned vw[4] = {vw0, vw0 + 23552, vw0 + 2 * 23552, vw0 + 3 * 23552};
            const unsigned sxb = p * 65536, swb = j * 128;
            const i32x4 s0 = mk_srd(x, 0);
            __builtin_amdgcn_s_setprio(3);
#define LAG 40
#define GRP(n, g) do { const unsigned kc_ = (P_CHUNK[n] + rot) & 3; \
            if (P_KIND[n] == 0) dma4(sx, vx, sxb + kc_ * 256 + ((g) >> 1) * 128 + ((g) & 1) * 32768, P_SLOT[n] * SLOT + (g) * 4096); \
            else dma4(sw, vw, swb + kc_ * (128 * 3072) + (g) * (32 * 3072) + (P_KIND[n] % 3) * 1024, P_SLOT[n] * SLOT + (g) * 4096); } while (0)
#define ISSUE(n) do { wait_vm<LAG>(); GRP(n, 0); GRP(n, 1); GRP(n, 2); GRP(n, 3); dma_zero(s0, L_LAND + 60 - 4 * (n)); if ((n) < 10) ASTAMP((n) % 10); } while (0)
            ASTAMP(0);
            dma(mk_srd(wr, D_ * 2), ((lane + 16 * rot) & 63) * 16, 0, L_WR);
            GRP(0, 0); GRP(0, 1); GRP(0, 2); GRP(0, 3);
            asm volatile("s_barrier" ::: "memory");
            dma_zero(s0, L_LAND + 60);
            ISSUE(1); ISSUE(2); ISSUE(3); ISSUE(4); ISSUE(5); ISSUE(6); ISSUE(7);
            asm volatile("s_barrier" ::: "memory");
            ISSUE(8); ISSUE(9); ISSUE(10); ISSUE(11); ISSUE(12); ISSUE(13); ISSUE(14); ISSUE(15);
            AFLUSH(10, 16);
            tag = rfl(ldsv<unsigned>(W_TAG));
        } else if (lw == 1) {
            const unsigned ep = __hip_atomic_load((gu32*)(ws + WS_EPOCH), RLX_AGENT);
            if (lane < 32) ldsw<unsigned>(L_WORDS + lane * 4, 0u);
            if (lane < 16) ldsw<unsigned>(L_LAND + lane * 4, 1u);
            ldsw<u32x4>(L_ONES + lane * 16, (u32x4){0x3f803f80u, 0x3f803f80u, 0x3f803f80u, 0x3f803f80u});
            ldsw<u32x4>(L_ONES + 1024 + lane * 16, (u32x4){0x3f803f80u, 0x3f803f80u, 0x3f803f80u, 0x3f803f80u});
            ldsw<u32x4>(L_CSP + 512 + lane * 16, (u32x4){~0u, ~0u, ~0u, ~0u});
            bar_lds();
            tag = rfl(ep) + 1u;
            lds_flag(W_TAG, tag);
            bar_lds();
        } else {
            const int sv = lw - 2, row = lane & 15, g = lane >> 4;
            unsigned xa0 = (4 * sv + (row >> 3)) * 1024 + (row & 7) * 128 + ((g ^ (row >> 1)) * 16), xa1 = xa0 ^ 64;
            unsigned aa = ((row & 3) == 1 ? L_WR : L_ONES) + g * 16, a_full = L_LAND;
            PIN(xa0); PIN(xa1); PIN(aa); PIN(a_full);
            f32x4 sA = {}, sB = {}, gA = {}, gB = {};
            float sw_ = 0.f;
            asm volatile("s_barrier" ::: "memory");
            bf16x8 af[16], xa[4], xb_[4], ya[4], yb[4];
#define ST_RD(A, B, i) do { _Pragma("unroll") for (int s = 0; s < 4; ++s) { \
                A[s] = ldsr<bf16x8>(((s & 1) ? xa1 : xa0) + (i) * SLOT + (s >> 1) * 8192); B[s] = ldsr<bf16x8>(((s & 1) ? xa1 : xa0) + (i) * SLOT + (s >> 1) * 8192 + 2048); SBAR(); } } while (0)
#define ST_MM(A, B, i) do { _Pragma("unroll") for (int s = 0; s < 4; ++s) { gA = MFMA16(A[s], A[s], gA); sA = MFMA16(af[4 * (i) + s], A[s], sA); gB = MFMA16(B[s], B[s], gB); sB = MFMA16(af[4 * (i) + s], B[s], sB); \
                asm volatile("" : "+v"(gA), "+v"(gB), "+v"(sA), "+v"(sB)); SBAR(); } } while (0)
            land_wait<0>(a_full); ASTAMP(0);
            ST_RD(xa, xb_, 0);
#pragma unroll
            for (int s = 0; s < 16; ++s) af[s] = ldsr<bf16x8>(aa + s * 64);
            const u32x4 w4 = ldsr<u32x4>(L_WR + lane * 16);
            land_wait<1>(a_full); ASTAMP(1);
            ST_RD(ya, yb, 1);
            ST_MM(xa, xb_, 0); ST_MM(ya, yb, 1);
            sw_ = wave_sum(dot2(w4.x, 0x3f803f80u, dot2(w4.y, 0x3f803f80u, dot2(w4.z, 0x3f803f80u, dot2(w4.w, 0x3f803f80u, 0.f))))); PIN(sw_); SBAR();
            ASTAMP(2); land_wait<3>(a_full); ASTAMP(3);
            ST_RD(xa, xb_, 2); ST_MM(xa, xb_, 2);
            ASTAMP(4); land_wait<5>(a_full); ASTAMP(5);
            ST_RD(ya, yb, 3); ST_MM(ya, yb, 3); ASTAMP(6);
            const int e = lane & 3;
            const float dA = e == 0 ? gA[0] : e == 1 ? gA[1] : e == 2 ? gA[2] : gA[3], dB = e == 0 ? gB[0] : e == 1 ? gB[1] : e == 2 ? gB[2] : gB[3];
            const bool ownA = g == (row >> 2), ownB = (g ^ 1) == (row >> 2);
            const auto sw16 = __builtin_amdgcn_permlane16_swap(__float_as_uint(dB), __float_as_uint(dB), false, false);
            const float ssq = ownA ? dA : __uint_as_float((g & 1) ? sw16[0] : sw16[1]);
            const float mean = (ownA ? sA[0] : sB[0]) * (1.f / D_), var = fmaxf(ssq * (1.f / D_) - mean * mean, 0.f);
            const float mb = rbf(mean), rstd = __builtin_amdgcn_rcpf(rbf(__builtin_amdgcn_sqrtf(rbf(rbf(var) + 1e-5f))));
            const int tk = 32 * sv + (ownA ? 0 : 16) + row;
            if (ownA || ownB) { ldsw<float>(L_MEAN + tk * 4, mb); ldsw<float>(L_RSTD + tk * 4, rstd); }
            ASTAMP(7);
            bar_lds();
            ASTAMP(8); AFLUSH(9, 16);
            tag = rfl(ldsv<unsigned>(W_TAG));
            if (j == ((p + 4 * sv) & 7)) {
                const float rv = ((ownA ? sA[1] : sB[1]) - mb * sw_) * rstd;
                const unsigned bits = pk2(rv, rv) & 0xffffu;
                const unsigned k1 = ((bits ^ ((bits & 0x8000u) ? 0xffffu : 0x8000u)) << 16) | (unsigned)(2047 - (64 * p + tk));
                const unsigned kmax = wave_max(ownA || ownB ? k1 : 0u);
                if (lane == 0) put_gran(ws, WS_RG + (2 * p + sv) * 128, tag, kmax);
            }
            land_wait<11, 4>(a_full);
        }
        STAMP(1);
        if (lw < 2) {
            lds_wait<1, 1>(a_freeall);
            if (p > 0) {
#pragma unroll
                for (int i = 0; i < 4; ++i) ldsw<u32x4>(L_ONES8 + lw * 4096 + i * 1024 + lane * 16, (u32x4){0x3f803f80u, 0x3f803f80u, 0x3f803f80u, 0x3f803f80u});
                lds_flag(W_ONES + 4 * lw, 1);
                const int g0 = p >= 4 ? 0 : 4 - p;
                {
                    const int g = max((lane >> 1) & 3, g0);
                    poll_flags(ws, WS_FV + (((p - 4 + g) * 8 + j) * 4 + 2 * lw + (lane & 1)) * 128, tag);
                }
                STAMP(2);
                u32x4 vv[16];
#pragma unroll
                for (int i = 0; i < 16; ++i) vv[i] = BLOAD(rs_ws, lane * 16, WS_V + (unsigned)((p - 4 + max(i >> 2, g0)) * 32 + 4 * j + 2 * lw) * 2048u + (i & 3) * 1024u, SC1);
                lds_wait<1>(W_ONES + 4 * (lane & 1));
#pragma unroll
                for (int i = 0; i < 16; ++i) ldsw<u32x4>(L_VREM + (2 * lw + ((i >> 1) & 1)) * VREM_STRIDE + (2 * (i >> 2) + (i & 1)) * 1024 + lane * 16, vv[i]);
                lds_flag(W_VRDY + 4 * lw, 1);
                STAMP(3);
            }
            const i32x4 so = mk_srd(wo, D_ * D_ * 2);
            const unsigned vo0 = r * 1024 + swz, vo[4] = {vo0, vo0 + 7168, vo0 + 2 * 7168, vo0 + 3 * 7168};
#pragma unroll
            for (int c = 0; c < 8; ++c) {
                const unsigned kc = (c + p) & 7;
                dma4(so, vo, (kc * 64 + lw * 32) * 1024 + j * 128, L_WO + kc * 8192 + lw * 4096);
            }
            wait_vm<0>();
            lds_flag(W_WORDY + 4 * lw, 1);
            STAMP(4);
        } else {
            const int sv = lw - 2, n1 = blk - 32;
            unsigned key;
#define ARGMAX() do { if (sv == 0) { unsigned kv_[1]; sweep<1, 4>(ws, WS_RG + lane * 128, 0, tag, kv_); key = wave_max(kv_[0]); lds_put(W_KEY, key); } \
            else key = lds_get<2>(W_KEY); } while (0)
            if (p < 4) {
                const int f0 = 43 * blk + 22 * sv, nf = 22 - sv;
                const auto rs_d = __builtin_amdgcn_make_buffer_rsrc((void*)wd, 0, F_ * D_ * 2, 0x00020000);
                u32x4 w[22];
#pragma unroll
                for (int i = 0; i < 22; ++i) w[i] = BLOAD(rs_d, lane * 16, (f0 + i) * 1024, 0);
                ARGMAX();
                STAMP(2);
#pragma unroll
                for (int i = 0; i < 22; i += 2) {
#pragma unroll
                    for (int d = 0; d < 4; ++d) { const unsigned a = w[i][d], c = w[i + 1][d]; w[i][d] = __builtin_amdgcn_perm(c, a, 0x05040100u); w[i + 1][d] = __builtin_amdgcn_perm(c, a, 0x07060302u); }
                    PIN(w[i]); PIN(w[i + 1]);
                }
                unsigned gv[16];
                const int fc = f0 + min(l31, nf - 1);
                sweep<16, 8>(ws, WS_G1 + (h * 16 * 1408 + (fc & ~127) + (fc & 1) * 64 + ((fc >> 1) & 3) * 16 + ((fc >> 3) & 15)) * 8, 1408 * 8, tag, gv);
                STAMP(3);
                float sum = 0.f;
#pragma unroll
                for (int k = 0; k < 16; ++k) sum += __uint_as_float(gv[k]);
                sum = rbf(sum);
                auto sw2 = __builtin_amdgcn_permlane32_swap(__float_as_uint(sum), __float_as_uint(sum), false, false);
                const float g = __uint_as_float(sw2[0]), u = __uint_as_float(sw2[1]);
                float hv = rbf(g / (1.f + __expf(-g))) * u;
                if (l31 >= nf) hv = 0.f;
                const unsigned hb = pk2(hv, hv) & 0xffffu;
                float y[8] = {};
#pragma unroll
                for (int i = 0; i < 22; i += 2) {
                    const unsigned h2 = (unsigned)__builtin_amdgcn_readlane((int)hb, i) | ((unsigned)__builtin_amdgcn_readlane((int)hb, i + 1) << 16);
#pragma unroll
                    for (int d = 0; d < 4; ++d) { y[2 * d] = dot2(w[i][d], h2, y[2 * d]); y[2 * d + 1] = dot2(w[i + 1][d], h2, y[2 * d + 1]); }
                }
#pragma unroll
                for (int e = 0; e < 8; ++e) put_gran(ws, WS_G2 + ((blk * 2 + sv) * 512 + e * 64 + lane) * 8, tag, __float_as_uint(y[e]));
                STAMP(4);
            } else if (n1 < 176) {
                const int cg = n1 % 11, kg = n1 / 11, rr = lane >> 4, c = lane & 15;
                const auto rs_g = __builtin_amdgcn_make_buffer_rsrc((void*)(sv ? wu : wg), 0, D_ * F_ * 2, 0x00020000);
                u32x4 w[8];
#pragma unroll
                for (int i = 0; i < 8; ++i) w[i] = BLOAD(rs_g, rr * (8 * F_ * 2) + c * 16, (32 * kg + i) * (F_ * 2) + cg * 256, 0);
                ARGMAX();
                STAMP(2);
                const int idx = 2047 - (int)(key & 0xffffu);
                const u32x4 xr = BLOAD(rs_x, lane * 16, idx * 1024, 0), xo = BLOAD(rs_x, rr * 16, idx * 1024 + kg * 64, 0);
#pragma unroll
                for (int i = 0; i < 8; i += 2) {
#pragma unroll
                    for (int d = 0; d < 4; ++d) { const unsigned a = w[i][d], e = w[i + 1][d]; w[i][d] = __builtin_amdgcn_perm(e, a, 0x05040100u); w[i + 1][d] = __builtin_amdgcn_perm(e, a, 0x07060302u); }
                    PIN(w[i]); PIN(w[i + 1]);
                }
                float s1 = dot2(xr.x, 0x3f803f80u, dot2(xr.y, 0x3f803f80u, dot2(xr.z, 0x3f803f80u, dot2(xr.w, 0x3f803f80u, 0.f))));
                float s2 = dot2(xr.x, xr.x, dot2(xr.y, xr.y, dot2(xr.z, xr.z, dot2(xr.w, xr.w, 0.f))));
                s1 = wave_sum(s1); s2 = wave_sum(s2);
                const float mean = s1 * (1.f / D_), var = fmaxf(s2 * (1.f / D_) - mean * mean, 0.f);
                const float mb = rbf(mean), rstd = 1.f / rbf(__builtin_sqrtf(rbf(rbf(var) + 1e-5f)));
                float a[8] = {};
#pragma unroll
                for (int i = 0; i < 4; ++i) {
                    const unsigned d2 = pk2(blo(xo[i]) - mb, bhi(xo[i]) - mb), xn2 = pk2(blo(d2) * rstd, bhi(d2) * rstd);
#pragma unroll
                    for (int d = 0; d < 4; ++d) { a[2 * d] = dot2(w[2 * i][d], xn2, a[2 * d]); a[2 * d + 1] = dot2(w[2 * i + 1][d], xn2, a[2 * d + 1]); }
                }
#pragma unroll
                for (int e = 0; e < 8; ++e) a[e] = swap32add(swap16add(a[e]));
                const float e0 = rr == 0 ? a[0] : rr == 1 ? a[2] : rr == 2 ? a[4] : a[6], e1 = rr == 0 ? a[1] : rr == 1 ? a[3] : rr == 2 ? a[5] : a[7];
                const unsigned go = WS_G1 + ((sv * 16 + kg) * 1408 + cg * 128 + lane) * 8;
                put_gran(ws, go, tag, __float_as_uint(e0)); put_gran(ws, go + 512, tag, __float_as_uint(e1));
                STAMP(4);
            } else {
                ARGMAX();
            }
            if (((2047 - (int)(key & 0xffffu)) >> 6) == p) {
                float ys = 0.f;
#pragma unroll 1
                for (int hf = 0; hf < 2; ++hf) {
                    unsigned gv[16];
                    sweep<16, 8>(ws, WS_G2 + ((sv * 32 + hf * 16) * 512 + (lane >> 3) * 64 + 8 * j + (lane & 7)) * 8, 4096, tag, gv);
#pragma unroll
                    for (int k = 0; k < 16; ++k) ys += __uint_as_float(gv[k]);
                }
                ldsw<float>(L_Y + sv * 256 + ((lane & 7) * 8 + (lane >> 3)) * 4, ys);
            }
            STAMP(5);
            if (b == 0 && sv == 1) {
#pragma unroll 1
                for (int i = 0; i < 4; ++i) poll_flags(ws, WS_FK + (i * 64 + lane) * 512, tag);
                if (lane == 0) put_flag(ws, WS_EPOCH, tag);
            }
        }
        EPI_PREP();
    } else {
        const int t = wid & 1, hp = wid >> 1, pm = t << 1;
        unsigned xb[4], wl[4], wh[4];
        {
            const int row = 32 * t + l31, q = (lane & 15) >> 2, pp = lane & 3, blkc = (lane >> 4) & 1;
            const unsigned wphys = (unsigned)((4 * hp + 2 * blkc + (pp >> 1)) ^ (((q >> 1) & 1) << 2));
#pragma unroll
            for (int c = 0; c < 4; ++c) {
                xb[c] = (row >> 3) * 1024 + (row & 7) * 128 + (((2 * (c ^ pm) + h) ^ ((row >> 1) & 7)) * 16);
                PIN(xb[c]);
            }
#pragma unroll
            for (int c = 0; c < 4; c += 2) {
                wl[c] = (2 * (c ^ pm) + h) * 1024 + q * 128 + wphys * 16 + 8 * (pp & 1);
                wh[c] = wl[c] + 65536;
                PIN(wl[c]); PIN(wh[c]);
                wl[c + 1] = wl[c] + 2048; wh[c + 1] = wh[c] + 2048;
            }
        }
        const unsigned one2 = (lane & 7) == 4 * ((lane >> 4) & 1) ? 0x3f803f80u : 0u;
        bf16x8 csel = asfrag((u32x4){one2, one2, one2, one2});
        unsigned a_full = L_LAND, a_mean = L_MEAN + (32 * t + l31) * 4, a_csw = L_CSP + wid * 128 + l31 * 4, a_csr = L_CSP + (2 * hp + h) * 128 + l31 * 4;
        unsigned l16 = lane * 16;
        PIN(csel); PIN(a_full); PIN(a_mean); PIN(a_csw); PIN(a_csr); PIN(l16);
        const unsigned g_kv = (unsigned)(p * 32 + 4 * j + 2 * hp) * 2048u + t * 1024u;
        bf16x8 xf[32], wa[8], wbf[8];
        f32x16 acc = {};
        f32x4 cs4 = {};
        unsigned fullv, fl;
        __builtin_amdgcn_s_setprio(1);
        asm volatile("s_barrier" ::: "memory");

#define WADDR(pos, s) ((P_SLOT[pos] < 4 ? wl[(s) & 3] : wh[(s) & 3]) + (P_SLOT[pos] & 3) * SLOT + ((s) >> 2) * 8192)
#define RD1(W, pos, s) do { W[s] = ldtr2(WADDR(pos, s), WADDR(pos, s) + 512); SBAR(); } while (0)
#define M1(SW, W, i, s) do { acc = (SW) ? MFMA32(W[s], xf[8 * (i) + (s)], acc) : MFMA32(xf[8 * (i) + (s)], W[s], acc); PIN(acc); SBAR(); } while (0)
#define C1(W, s) do { cs4 = MFMA16(csel, W[s], cs4); PIN(cs4); SBAR(); } while (0)
#define RX2(i, s) do { xf[8 * (i) + (s)] = ldsr<bf16x8>(xb[(s) & 3] + (i) * SLOT + ((s) >> 2) * 8192); xf[8 * (i) + (s) + 1] = ldsr<bf16x8>(xb[((s) + 1) & 3] + (i) * SLOT + ((s) >> 2) * 8192); SBAR(); } while (0)
#define RD_W(W, pos) do { RD1(W, pos, 0); RD1(W, pos, 1); RD1(W, pos, 4); RD1(W, pos, 5); RD1(W, pos, 2); RD1(W, pos, 3); RD1(W, pos, 6); RD1(W, pos, 7); } while (0)
#define K_STAGE(i, pos) do { ASTAMP(2 * (i) + 1); land_wait<pos>(a_full); ASTAMP(2 * (i) + 2); RD_W(wa, pos); \
        M1(1, wa, i, 0); C1(wa, 0); M1(1, wa, i, 1); C1(wa, 1); RX2((i) + 1, 0); M1(1, wa, i, 4); RX2((i) + 1, 4); C1(wa, 4); M1(1, wa, i, 5); RX2((i) + 1, 2); C1(wa, 5); \
        M1(1, wa, i, 2); RX2((i) + 1, 6); M1(1, wa, i, 3); M1(1, wa, i, 6); M1(1, wa, i, 7); } while (0)
#define PEEK(pos) do { fullv = ldsv<unsigned>(a_full + 60 - 4 * (pos)); SBAR(); } while (0)
#define MMN(SW, W, i, Wn, pos) do { M1(SW, W, i, 0); land_chk<pos>(fullv, a_full); SBAR(); if ((pos) < 15) PEEK((pos) + 1); \
        RD1(Wn, pos, 0); RD1(Wn, pos, 1); M1(SW, W, i, 1); RD1(Wn, pos, 4); RD1(Wn, pos, 5); M1(SW, W, i, 4); RD1(Wn, pos, 2); RD1(Wn, pos, 3); M1(SW, W, i, 5); RD1(Wn, pos, 6); RD1(Wn, pos, 7); \
        M1(SW, W, i, 2); M1(SW, W, i, 3); C1(Wn, 0); C1(Wn, 1); M1(SW, W, i, 6); C1(Wn, 4); C1(Wn, 5); M1(SW, W, i, 7); } while (0)
#define MEAN_FIX(SW, c_) do { const float ch_ = rbf(c_); const bf16x8 cf_ = asfrag((u32x4){pk2(ch_, c_ - ch_), 0u, 0u, 0u}); \
        acc = (SW) ? MFMA32(cf_, mfrag, acc) : MFMA32(mfrag, cf_, acc); PIN(acc); SBAR(); } while (0)

        land_wait<0>(a_full);
        ASTAMP(0);
        RX2(0, 0); RX2(0, 4); RX2(0, 2); RX2(0, 6);
        K_STAGE(0, 2);
        K_STAGE(1, 4);
        ASTAMP(5); land_wait<6>(a_full); ASTAMP(6);
        RD_W(wa, 6);
        M1(1, wa, 2, 0); C1(wa, 0); RX2(3, 0); M1(1, wa, 2, 1); RX2(3, 4); C1(wa, 1); M1(1, wa, 2, 4); RX2(3, 2); C1(wa, 4); RX2(3, 6);
        PEEK(7);
        M1(1, wa, 2, 5); C1(wa, 5); M1(1, wa, 2, 2); M1(1, wa, 2, 3);
        land_chk<7>(fullv, a_full); SBAR(); ASTAMP(7);
        RD1(wbf, 7, 0); RD1(wbf, 7, 1); RD1(wbf, 7, 4); M1(1, wa, 2, 6); RD1(wbf, 7, 5); RD1(wbf, 7, 2); RD1(wbf, 7, 3); M1(1, wa, 2, 7); RD1(wbf, 7, 6); RD1(wbf, 7, 7);
        C1(wbf, 0); C1(wbf, 1); C1(wbf, 4); C1(wbf, 5); M1(1, wbf, 3, 0); M1(1, wbf, 3, 1);
        ldsw<float>(a_csw, cs4[0]); SBAR();
        M1(1, wbf, 3, 4); M1(1, wbf, 3, 5);
        bar_lds();
        ASTAMP(8);
        float cs_ = ldsv<float>(a_csr);
        const float mb = ldsv<float>(a_mean), rstd = ldsv<float>(a_mean + 256);
        fl = ldsv<unsigned>(W_TAG); SBAR();
        M1(1, wbf, 3, 2); M1(1, wbf, 3, 3); M1(1, wbf, 3, 6);
        bf16x8 mfrag = asfrag((u32x4){pk2(-mb, -mb), 0u, 0u, 0u}); PIN(mfrag); SBAR();
        M1(1, wbf, 3, 7);
        MEAN_FIX(1, cs_);
        const unsigned tag = rfl(fl);
        PEEK(8);
        acc *= rstd;
        const u32x4 k0 = pack8(acc, 0), k1 = pack8(acc, 8);
        BSTORE(k0, rs_ws, l16, WS_K + g_kv, SC1); BSTORE(k1, rs_ws, l16, WS_K + g_kv + 2048, SC1);
        SBAR();
        ASTAMP(9); AFLUSH(10, 16);
        STAMP(2);
        land_chk<8>(fullv, a_full);
        RD_W(wbf, 8); PEEK(9);
        acc = (f32x16){}; cs4 = (f32x4){};
        C1(wbf, 0); C1(wbf, 1); C1(wbf, 4); C1(wbf, 5);
        MMN(0, wbf, 0, wa, 9); MMN(0, wa, 1, wbf, 10); MMN(0, wbf, 2, wa, 11);
        f32x4 rs4[4];
        M1(0, wa, 3, 0);
        ldsw<float>(a_csw + 512, cs4[0]); SBAR();
        land_chk<12>(fullv, a_full); SBAR(); PEEK(13);
        M1(0, wa, 3, 1); RD1(wbf, 12, 0); RD1(wbf, 12, 1); M1(0, wa, 3, 4); RD1(wbf, 12, 4); RD1(wbf, 12, 5); M1(0, wa, 3, 5); RD1(wbf, 12, 2); RD1(wbf, 12, 3);
        M1(0, wa, 3, 2); RD1(wbf, 12, 6); RD1(wbf, 12, 7);
        M1(0, wa, 3, 3);
        cs_ = ldsv<float>(a_csr + 512);
#pragma unroll
        for (int g = 0; g < 4; ++g) rs4[g] = ldsr<f32x4>(L_RSTD + (32 * t + 8 * g + 4 * h) * 4);
        SBAR();
        M1(0, wa, 3, 6); M1(0, wa, 3, 7);
        cs4 = (f32x4){};
        C1(wbf, 0); C1(wbf, 1); C1(wbf, 4); C1(wbf, 5);
        val_chk<512>(cs_, a_csr);
        MEAN_FIX(0, cs_);
        drain();
        if (lane == 0) put_flag(ws, WS_FK + (blk * 4 + wid) * 128, tag);
        STAMP(3);
#pragma unroll
        for (int r = 0; r < 16; ++r) acc[r] *= rs4[r >> 2][r & 3];
        const u32x4 v0 = pack8(acc, 0), v1 = pack8(acc, 8);
        const unsigned vo = (l31 >> 4) * 2048 + h * 256 + (lane & 15) * 16;
        BSTORE(v0, rs_ws, vo, WS_V + g_kv, SC1); BSTORE(v1, rs_ws, vo, WS_V + g_kv + 512, SC1);
        SBAR();
        STAMP(4);
        acc = (f32x16){};
        MMN(1, wbf, 0, wa, 13); MMN(1, wa, 1, wbf, 14); MMN(1, wbf, 2, wa, 15);
        M1(1, wa, 3, 0);
        ldsw<float>(a_csw + 1024, cs4[0]); lds_flag(W_FREE + 4 * wid, 1); SBAR();
        M1(1, wa, 3, 1);
        ldsw<u32x4>(L_KOWN + (4 * hp + t) * 1024 + l16, k0); ldsw<u32x4>(L_KOWN + (4 * hp + 2 + t) * 1024 + l16, k1); SBAR();
        M1(1, wa, 3, 4);
        ldsw<u32x4>(L_VOWN + (4 * hp + t) * 1024 + vo, v0); ldsw<u32x4>(L_VOWN + (4 * hp + t) * 1024 + vo + 512, v1); SBAR();
        M1(1, wa, 3, 5); M1(1, wa, 3, 2);
        cs_ = ldsv<float>(a_csr + 1024); SBAR();
        M1(1, wa, 3, 3); M1(1, wa, 3, 6); M1(1, wa, 3, 7);
        val_chk<1024>(cs_, a_csr);
        MEAN_FIX(1, cs_);
        drain();
        if (lane == 0) put_flag(ws, WS_FV + (blk * 4 + wid) * 128, tag);
        acc *= rstd * (0.25f * 1.4426950408889634f);
        ldsw<u32x4>(L_QOWN + (4 * hp + t) * 1024 + l16, pack8(acc, 0)); ldsw<u32x4>(L_QOWN + (4 * hp + 2 + t) * 1024 + l16, pack8(acc, 8));
        lds_flag(W_PAIR + 4 * wid, 1);
        lds_wait<1>(W_PAIR + 4 * (wid ^ 1));
        STAMP(5);
        const int head = 4 * j + wid;
        const bf16x8 q0 = ldsr<bf16x8>(L_QOWN + wid * 2048 + lane * 16), q1 = ldsr<bf16x8>(L_QOWN + wid * 2048 + 1024 + lane * 16);
        f32x16 o0 = {}, o1 = {};
        const unsigned vlane = h * 256 + (lane & 15) * 16;
#define EXPP(Sx, Pa, Pb) do { _Pragma("unroll") for (int r = 0; r < 16; ++r) Sx[r] = __builtin_amdgcn_exp2f(Sx[r]); Pa = asfrag(pack8(Sx, 0)); Pb = asfrag(pack8(Sx, 8)); } while (0)
#define EX4(E, Sx, r) do { E[r] = __builtin_amdgcn_exp2f(Sx[r]); E[r + 1] = __builtin_amdgcn_exp2f(Sx[r + 1]); E[r + 2] = __builtin_amdgcn_exp2f(Sx[r + 2]); E[r + 3] = __builtin_amdgcn_exp2f(Sx[r + 3]); \
        asm volatile("" : "+v"(E[r]), "+v"(E[r + 1]), "+v"(E[r + 2]), "+v"(E[r + 3])); SBAR(); } while (0)
#define PK(E, Pa, Pb) do { Pa = asfrag((u32x4){pk2(E[0], E[1]), pk2(E[2], E[3]), pk2(E[4], E[5]), pk2(E[6], E[7])}); \
        Pb = asfrag((u32x4){pk2(E[8], E[9]), pk2(E[10], E[11]), pk2(E[12], E[13]), pk2(E[14], E[15])}); asm volatile("" : "+v"(Pa), "+v"(Pb)); SBAR(); } while (0)
        f32x16 cm;
        {
#pragma unroll
            for (int r = 0; r < 16; ++r) cm[r] = ((r & 3) + 8 * (r >> 2) + 4 * h <= l31) ? 0.f : -INFINITY;
            const bf16x8 k8 = ldsr<bf16x8>(L_KOWN + wid * 2048 + lane * 16), k9 = ldsr<bf16x8>(L_KOWN + wid * 2048 + 1024 + lane * 16);
            const unsigned va = (l31 < 16 ? L_VOWN + wid * 2048 : L_ONES) + vlane;
            const bf16x8 v8a = ldsr<bf16x8>(va), v8b = ldsr<bf16x8>(va + 512), v9a = ldsr<bf16x8>(va + 1024), v9b = ldsr<bf16x8>(va + 1536);
            f32x16 s0 = MFMA32(k8, q0, cm), s1 = MFMA32(k8, q1, (f32x16){}), s2 = MFMA32(k9, q1, cm);
            SBAR();
            bf16x8 pa, pb;
            EXPP(s0, pa, pb); o0 = MFMA32(v8a, pa, o0); o0 = MFMA32(v8b, pb, o0); SBAR();
            EXPP(s1, pa, pb); o1 = MFMA32(v8a, pa, o1); o1 = MFMA32(v8b, pb, o1); SBAR();
            EXPP(s2, pa, pb); o1 = MFMA32(v9a, pa, o1); o1 = MFMA32(v9b, pb, o1); SBAR();
#pragma unroll
            for (int r = 0; r < 16; ++r) cm[r] = ((r & 3) + 8 * (r >> 2) + 4 * h > l31) ? 0.f : -INFINITY;
            PIN(cm); PIN(o0); PIN(o1);
        }
        STAMP(6);
        unsigned va = (l31 < 16 ? L_VREM + wid * VREM_STRIDE : L_ONES8) + vlane, a_vrdy = W_VRDY + 4 * hp;
        const f32x16 z16 = {};
        if (p >= 4) {
            unsigned sk = WS_K + (unsigned)((p - 4) * 32 + head) * 2048u, sk1 = sk + 65536, sk2 = sk + 2 * 65536, sk3 = sk + 3 * 65536;
            unsigned fo = WS_FK + (((p - 4 + ((lane >> 1) & 3)) * 8 + j) * 4 + 2 * hp + (lane & 1)) * 128;
            PIN(va); PIN(a_vrdy); PIN(fo);
            asm volatile("" : "+s"(sk), "+s"(sk1), "+s"(sk2), "+s"(sk3));
            poll_flags(ws, fo, tag);
            STAMP(7);
            bf16x8 kf[8], p0a[8], p0b[8];
            kf[0] = asfrag(BLOAD(rs_ws, l16, sk, SC1));  kf[1] = asfrag(BLOAD(rs_ws, l16 + 1024, sk, SC1));  kf[2] = asfrag(BLOAD(rs_ws, l16, sk1, SC1)); kf[3] = asfrag(BLOAD(rs_ws, l16 + 1024, sk1, SC1));
            kf[4] = asfrag(BLOAD(rs_ws, l16, sk2, SC1)); kf[5] = asfrag(BLOAD(rs_ws, l16 + 1024, sk2, SC1)); kf[6] = asfrag(BLOAD(rs_ws, l16, sk3, SC1)); kf[7] = asfrag(BLOAD(rs_ws, l16 + 1024, sk3, SC1));
            SBAR();
            f32x16 sA, sB;
            float e[16];
            sA = MFMA32(kf[0], q0, cm); PIN(sA); SBAR();
#define UA(i, Sc, Sn, NEXT) do { Sn = NEXT; PIN(Sn); SBAR(); EX4(e, Sc, 0); EX4(e, Sc, 4); EX4(e, Sc, 8); EX4(e, Sc, 12); PK(e, p0a[i], p0b[i]); } while (0)
            UA(0, sA, sB, MFMA32(kf[1], q0, z16)); UA(1, sB, sA, MFMA32(kf[2], q0, z16)); UA(2, sA, sB, MFMA32(kf[3], q0, z16)); UA(3, sB, sA, MFMA32(kf[4], q0, z16));
            UA(4, sA, sB, MFMA32(kf[5], q0, z16)); UA(5, sB, sA, MFMA32(kf[6], q0, z16)); UA(6, sA, sB, MFMA32(kf[7], q0, z16));
            fl = ldsv<unsigned>(a_vrdy);
            UA(7, sB, sA, MFMA32(kf[1], q1, cm));
            STAMP(8);
            lds_chk<1>(fl, a_vrdy);
            STAMP(9);
            bf16x8 pa, pb, vA0, vA1, vB0, vB1;
            vA0 = ldsr<bf16x8>(va); vA1 = ldsr<bf16x8>(va + 512);
#define UB(i, Sc, Sn, NEXT, Vc0, Vc1, Vn0, Vn1) do { \
                Vn0 = ldsr<bf16x8>(va + (i) * 1024); Vn1 = ldsr<bf16x8>(va + (i) * 1024 + 512); if ((i) < 7) { Sn = NEXT; PIN(Sn); } SBAR(); \
                EX4(e, Sc, 0);  if ((i) > 1) { o1 = MFMA32(Vc0, pa, o1); PIN(o1); SBAR(); } \
                EX4(e, Sc, 4);  if ((i) > 1) { o1 = MFMA32(Vc1, pb, o1); PIN(o1); SBAR(); } \
                EX4(e, Sc, 8);  o0 = MFMA32(Vc0, p0a[(i) - 1], o0); PIN(o0); SBAR(); \
                EX4(e, Sc, 12); o0 = MFMA32(Vc1, p0b[(i) - 1], o0); PIN(o0); SBAR(); \
                PK(e, pa, pb); } while (0)
            UB(1, sA, sB, MFMA32(kf[2], q1, z16), vA0, vA1, vB0, vB1);
            UB(2, sB, sA, MFMA32(kf[3], q1, z16), vB0, vB1, vA0, vA1);
            UB(3, sA, sB, MFMA32(kf[4], q1, z16), vA0, vA1, vB0, vB1);
            UB(4, sB, sA, MFMA32(kf[5], q1, z16), vB0, vB1, vA0, vA1);
            UB(5, sA, sB, MFMA32(kf[6], q1, z16), vA0, vA1, vB0, vB1);
            UB(6, sB, sA, MFMA32(kf[7], q1, z16), vB0, vB1, vA0, vA1);
            UB(7, sA, sB, z16, vA0, vA1, vB0, vB1);
            o0 = MFMA32(vB0, p0a[7], o0); o0 = MFMA32(vB1, p0b[7], o0); o1 = MFMA32(vB0, pa, o1); o1 = MFMA32(vB1, pb, o1);
        } else if (p > 0) {
            poll_flags(ws, WS_FK + ((min((lane >> 1) & 3, p - 1) * 8 + j) * 4 + 2 * hp + (lane & 1)) * 128, tag);
            bf16x8 kf[6];
#pragma unroll
            for (int i = 0; i < 6; ++i) kf[i] = asfrag(BLOAD(rs_ws, l16 + (i & 1) * 1024, WS_K + (unsigned)(max(p - 3 + (i >> 1), 0) * 32 + head) * 2048u, SC1));
            lds_wait<1>(a_vrdy);
#pragma unroll
            for (int i = 0; i < 6; ++i) {
                if (p - 3 + (i >> 1) >= 0) {
                    const bf16x8 va_ = ldsr<bf16x8>(va + (i + 2) * 1024), vb_ = ldsr<bf16x8>(va + (i + 2) * 1024 + 512);
                    f32x16 s0 = MFMA32(kf[i], q0, z16), s1 = MFMA32(kf[i], q1, z16);
                    bf16x8 pa, pb;
                    EXPP(s0, pa, pb); o0 = MFMA32(va_, pa, o0); o0 = MFMA32(vb_, pb, o0);
                    EXPP(s1, pa, pb); o1 = MFMA32(va_, pa, o1); o1 = MFMA32(vb_, pb, o1);
                }
            }
        }
        const bool safe = o0[8] >= 8.7e-19f && o0[8] <= 1.15e18f && o1[8] >= 8.7e-19f && o1[8] <= 1.15e18f;
        if (__builtin_expect(!__all(safe), 0)) {
            poll_flags(ws, ((lane & 2) ? WS_FV : WS_FK) + (blk * 4 + 2 * hp + (lane & 1)) * 128, tag);
#pragma unroll 1
            for (int qt = 0; qt < 2; ++qt) {
                const bf16x8 qq = qt ? q1 : q0;
                float m = -INFINITY;
                f32x16 oo = {};
#pragma unroll 1
                for (int pass = 0; pass < 2; ++pass) {
#pragma unroll 1
                    for (int i = 0; i <= 8; ++i) {
                        const int kt = 2 * p + qt - 8 + i;
                        if (kt < 0) continue;
                        const unsigned go = (unsigned)((kt >> 1) * 32 + head) * 2048u + (kt & 1) * 1024u;
                        f32x16 s = MFMA32(asfrag(BLOAD(rs_ws, lane * 16, WS_K + go, SC1)), qq, (f32x16){});
#pragma unroll
                        for (int r = 0; r < 16; ++r) { const int dk = l31 + 32 * (8 - i) - ((r & 3) + 8 * (r >> 2) + 4 * h); if (dk < 0 || dk >= 256) s[r] = -INFINITY; }
                        if (pass == 0) {
#pragma unroll
                            for (int r = 0; r < 16; ++r) m = fmaxf(m, s[r]);
                        } else {
                            u32x4 va_ = BLOAD(rs_ws, vlane, WS_V + go, SC1), vb_ = BLOAD(rs_ws, vlane, WS_V + go + 512, SC1);
                            if (l31 >= 16) va_ = vb_ = (u32x4){0x3f803f80u, 0x3f803f80u, 0x3f803f80u, 0x3f803f80u};
#pragma unroll
                            for (int r = 0; r < 16; ++r) s[r] -= m;
                            bf16x8 pa, pb; EXPP(s, pa, pb);
                            oo = MFMA32(asfrag(va_), pa, oo); oo = MFMA32(asfrag(vb_), pb, oo);
                        }
                    }
                    if (pass == 0) { auto r = __builtin_amdgcn_permlane32_swap(__float_as_uint(m), __float_as_uint(m), false, false); m = fmaxf(__uint_as_float(r[0]), __uint_as_float(r[1])); }
                }
                if (qt) o1 = oo; else o0 = oo;
            }
        }
        {
            const float i0 = __builtin_amdgcn_rcpf(o0[8]), i1 = __builtin_amdgcn_rcpf(o1[8]);
#pragma unroll
            for (int r = 0; r < 8; ++r) { o0[r] *= i0; o1[r] *= i1; }
            BSTORE(pack8(o0, 0), rs_ws, lane * 16, WS_O + (unsigned)(p * 64 + head) * 1024u, SC1);
            BSTORE(pack8(o1, 0), rs_ws, lane * 16, WS_O + (unsigned)(p * 64 + 32 + head) * 1024u, SC1);
            STAMP(10);
            drain();
            if (lane == 0) put_flag(ws, WS_FO + (blk * 4 + wid) * 128, tag);
        }
        STAMP(11);
        {
            lds_wait<1>(W_WORDY + 4 * (lane & 1));
            const int q = (lane & 15) >> 2, pp = lane & 3, blkc = (lane >> 4) & 1;
            const unsigned ob0 = L_WO + hp * 32768 + (4 * h + q) * 128 + (((2 * blkc + (pp >> 1)) ^ (((q >> 1) & 1) << 2)) * 16) + 8 * (pp & 1), ob1 = ob0 ^ 64;
            bf16x8 w0[16], w1[16], of[16];
#pragma unroll
            for (int i = 0; i < 16; ++i) { w0[i] = ldtr2(ob0 + i * 2048, ob0 + i * 2048 + 1024); w1[i] = ldtr2(ob1 + i * 2048, ob1 + i * 2048 + 1024); }
            EPI_PREP();
            poll_flags(ws, WS_FO + (p * 32 + 16 * hp + (lane & 15)) * 128, tag);
            STAMP(12);
#pragma unroll
            for (int i = 0; i < 16; ++i) of[i] = asfrag(BLOAD(rs_ws, lane * 16, WS_O + (unsigned)(p * 64 + 32 * t + 16 * hp + i) * 1024u, SC1));
            SBAR();
            f32x16 c0 = {}, c1 = {};
#pragma unroll
            for (int i = 0; i < 16; ++i) { c0 = MFMA32(w0[i], of[i], c0); c1 = MFMA32(w1[i], of[i], c1); PIN(c0); PIN(c1); SBAR(); }
            const unsigned pa = L_VREM + wid * VREM_STRIDE + l31 * 272 + h * 16;
#pragma unroll
            for (int g = 0; g < 4; ++g) ldsw<f32x4>(pa + g * 32, (f32x4){c0[4 * g], c0[4 * g + 1], c0[4 * g + 2], c0[4 * g + 3]});
#pragma unroll
            for (int g = 0; g < 4; ++g) ldsw<f32x4>(pa + 128 + g * 32, (f32x4){c1[4 * g], c1[4 * g + 1], c1[4 * g + 2], c1[4 * g + 3]});
        }
        STAMP(13);
    }
    asm volatile("s_waitcnt lgkmcnt(0)\n\ts_barrier" ::: "memory");
    STAMP(14);
    {
        const int tok = tid >> 3, cg = tid & 7;
        const unsigned pa = L_VREM + (tok >> 5) * VREM_STRIDE + (tok & 31) * 272 + cg * 32;
        const f32x4 a0 = ldsr<f32x4>(pa), a1 = ldsr<f32x4>(pa + 16), b0 = ldsr<f32x4>(pa + 2 * VREM_STRIDE), b1 = ldsr<f32x4>(pa + 2 * VREM_STRIDE + 16);
        const unsigned key = ldsr<unsigned>(W_KEY + 4);
        float av[8], xn[8];
#pragma unroll
        for (int e = 0; e < 4; ++e) { av[e] = a0[e] + b0[e]; av[4 + e] = a1[e] + b1[e]; }
#pragma unroll
        for (int d = 0; d < 4; ++d) { xn[2 * d] = blo(xnorm[d]); xn[2 * d + 1] = bhi(xnorm[d]); }
        if (__builtin_expect(2047 - (int)(key & 0xffffu) == 64 * p + tok, 0)) {
            const unsigned u = key >> 16, bits = u ^ ((u & 0x8000u) ? 0x8000u : 0xffffu);
            const float rs = rbf(1.f / (1.f + __expf(-blo(bits))));
#pragma unroll
            for (int e = 0; e < 8; ++e) xn[e] = rbf(xn[e] + rbf(rbf(ldsr<float>(L_Y + (cg * 8 + e) * 4) + ldsr<float>(L_Y + 256 + (cg * 8 + e) * 4)) * rs));
        }
        u32x4 ov;
#pragma unroll
        for (int d = 0; d < 4; ++d) {
            const unsigned a2 = pk2(av[2 * d], av[2 * d + 1]), s2 = pk2(blo(xchunk[d]) + blo(a2), bhi(xchunk[d]) + bhi(a2));
            ov[d] = pk2(blo(s2) + xn[2 * d], bhi(s2) + xn[2 * d + 1]);
        }
        *(u32x4*)((char*)out + (size_t)(64 * p + tok) * 1024 + j * 128 + cg * 16) = ov;
    }
    STAMP(15); CSTAMP(49);
}

extern "C" void kernel_launch(void* const* d_in, const int* in_sizes, int n_in, void* d_out, int out_size, void* d_ws, size_t ws_size,
                              hipStream_t stream) {
    (void)hipFuncSetAttribute((const void*)fused_block, hipFuncAttributeMaxDynamicSharedMemorySize, 163840);
    fused_block<<<256, 512, 163840, stream>>>((const u16*)d_in[0], (const u16*)d_in[1], (const u16*)d_in[2], (const u16*)d_in[4], (const u16*)d_in[5],
                                              (const u16*)d_in[6], (const u16*)d_in[7], (u16*)d_out, (char*)d_ws);
}
```

```cpp
#include <hip/hip_runtime.h>
#include <hip/hip_bf16.h>
#include <stdint.h>

typedef unsigned short u16;
typedef unsigned long long u64;
typedef short bf16x8 __attribute__((ext_vector_type(8)));
typedef short s16x4 __attribute__((ext_vector_type(4)));
typedef float f32x16 __attribute__((ext_vector_type(16)));
typedef float f32x4 __attribute__((ext_vector_type(4)));
typedef float f32x2 __attribute__((ext_vector_type(2)));
typedef unsigned u32x4 __attribute__((ext_vector_type(4)));
typedef unsigned u32x2 __attribute__((ext_vector_type(2)));
typedef int i32x4 __attribute__((ext_vector_type(4)));
typedef __bf16 bf2_t __attribute__((ext_vector_type(2)));
#define DI __device__ __forceinline__
#define LAS __attribute__((address_space(3)))
#define GAS __attribute__((address_space(1)))
typedef GAS unsigned gu32;
typedef GAS u64 gu64;
#define RLX_AGENT __ATOMIC_RELAXED, __HIP_MEMORY_SCOPE_AGENT
#define MFMA32(a, b, c) __builtin_amdgcn_mfma_f32_32x32x16_bf16((a), (b), (c), 0, 0, 0)
#define MFMA16(a, b, c) __builtin_amdgcn_mfma_f32_16x16x32_bf16((a), (b), (c), 0, 0, 0)
#define SBAR() __builtin_amdgcn_sched_barrier(0)
#define CFENCE() asm volatile("" ::: "memory")
#define PIN(x) asm volatile("" : "+v"(x))

#define DIAG 0

constexpr int S_ = 2048, D_ = 512, F_ = 1376;
constexpr unsigned WS_EPOCH = 0;
#define RGS 16
constexpr unsigned WS_RG = 4096;
constexpr unsigned WS_G1 = 16384;
constexpr unsigned WS_G2 = 393216;
constexpr unsigned WS_FV = 655360;
constexpr unsigned WS_FK = 7u << 20;
static_assert(WS_FV + 131072 <= (1u << 20), "ws map");
constexpr unsigned WS_K = 1u << 20;
constexpr unsigned WS_V = 3u << 20;
constexpr unsigned WS_O = 5u << 20;
constexpr unsigned WS_DIAG = 8u << 20;
constexpr unsigned WS_USED = 8u << 20;
constexpr unsigned SLOT = 16384;
constexpr unsigned L_WO = 65536;
constexpr unsigned L_VREM = 0, VREM_STRIDE = 8704;
constexpr unsigned L_ONES8 = L_VREM + 4 * VREM_STRIDE;
constexpr unsigned L_KOWN = 49152, L_QOWN = L_KOWN + 8192, L_VOWN = 9 * SLOT;
constexpr unsigned L_MISC = L_VOWN + 8192;
constexpr unsigned L_ONES = L_MISC;
constexpr unsigned L_LAND = L_MISC + 2048;
constexpr unsigned L_MEAN = L_LAND + 256, L_RSTD = L_MEAN + 256, L_JUNK = L_RSTD + 256;
constexpr unsigned L_WORDS = L_MISC + 3072;
constexpr unsigned L_CSP = L_MISC + 4096;
constexpr unsigned L_STP = L_CSP + 512;
constexpr unsigned L_XWP = L_STP + 1536;
constexpr unsigned L_WR = L_MISC + 7168;
constexpr unsigned L_Y = L_ONES8 + 8192;
constexpr unsigned L_PA = L_Y + 512, L_PB = 8 * SLOT;
static_assert(L_WR + 1024 <= 163840 && L_Y + 512 <= L_KOWN, "LDS");
constexpr unsigned W_FREE = L_WORDS, W_PAIR = L_WORDS + 32, W_VRDY = L_WORDS + 48, W_WORDY = L_WORDS + 56, W_KEY = L_WORDS + 64,
                   W_ONES = L_WORDS + 72, W_STAT = L_WORDS + 80, W_XW = L_WORDS + 96, W_YP = L_WORDS + 112, W_VISS = L_WORDS + 120, W_KEYV = L_WORDS + 128, W_VOWN = L_WORDS + 144, W_VSEEN = L_WORDS + 160, W_ODRN = L_WORDS + 176, W_KSEEN = L_WORDS + 192, W_PDONE = L_WORDS + 208  ;
constexpr unsigned SPIN_LDS = 1u << 20, SPIN_GLB = 1u << 16;

extern "C" __device__ float mul_np(float, float) __asm("llvm.amdgcn.fmul.legacy");
DI unsigned rfl(unsigned v) { return __builtin_amdgcn_readfirstlane(v); }
DI unsigned pk2(float a, float b) { f32x2 v = {a, b}; return __builtin_bit_cast(unsigned, __builtin_convertvector(v, bf2_t)); }
DI float rbf(float a) { return __uint_as_float(pk2(a, a) << 16); }
DI float blo(unsigned v) { return __uint_as_float(v << 16); }
DI float bhi(unsigned v) { return __uint_as_float(v & 0xffff0000u); }
DI float dot2(unsigned a, unsigned b, float c) { return __builtin_amdgcn_fdot2_f32_bf16(__builtin_bit_cast(bf2_t, a), __builtin_bit_cast(bf2_t, b), c, false); }
DI float swap32add(float v) { auto r = __builtin_amdgcn_permlane32_swap(__float_as_uint(v), __float_as_uint(v), false, false); return __uint_as_float(r[0]) + __uint_as_float(r[1]); }
#define DPPU(v, ctrl) ((unsigned)__builtin_amdgcn_update_dpp((int)(v), (int)(v), (ctrl), 0xf, 0xf, false))
#define DPPF(v, ctrl) __uint_as_float(DPPU(__float_as_uint(v), ctrl))
DI unsigned wave_max(unsigned k) {
    k = max(k, DPPU(k, 0xB1)); k = max(k, DPPU(k, 0x4E)); k = max(k, DPPU(k, 0x141)); k = max(k, DPPU(k, 0x140));
    return max(max((unsigned)__builtin_amdgcn_readlane((int)k, 0), (unsigned)__builtin_amdgcn_readlane((int)k, 16)),
               max((unsigned)__builtin_amdgcn_readlane((int)k, 32), (unsigned)__builtin_amdgcn_readlane((int)k, 48)));
}
DI float wave_sum(float v) {
    v += DPPF(v, 0xB1); v += DPPF(v, 0x4E); v += DPPF(v, 0x141); v += DPPF(v, 0x140);
    const int u = __float_as_int(v);
    return (__int_as_float(__builtin_amdgcn_readlane(u, 0)) + __int_as_float(__builtin_amdgcn_readlane(u, 16))) +
           (__int_as_float(__builtin_amdgcn_readlane(u, 32)) + __int_as_float(__builtin_amdgcn_readlane(u, 48)));
}
DI float swap16add(float v) { auto r = __builtin_amdgcn_permlane16_swap(__float_as_uint(v), __float_as_uint(v), false, false); return __uint_as_float(r[0]) + __uint_as_float(r[1]); }
template <class P> constexpr u64 lane_mask(P pred) { u64 m = 0; for (int l = 0; l < 64; ++l) m |= (u64)(pred(l) ? 1 : 0) << l; return m; }
#define LM(expr) ({ constexpr u64 m_ = lane_mask([](int l) { return (expr); }); __builtin_amdgcn_inverse_ballot_w64(m_); })
template <class T> DI T sel4(bool b0, bool b1, T a0, T a1, T a2, T a3) { const T lo = b0 ? a1 : a0, hi = b0 ? a3 : a2; return b1 ? hi : lo; }
template <class T> DI T ldsr(unsigned a) { return *(const LAS T*)(size_t)a; }
template <class T> DI void ldsw(unsigned a, T v) { *(LAS T*)(size_t)a = v; }
DI s16x4 ldtr(unsigned a) { typedef short v4s __attribute__((ext_vector_type(4))); return __builtin_amdgcn_ds_read_tr16_b64_v4i16((LAS v4s*)(size_t)a); }
DI bf16x8 ldtr2(unsigned a0, unsigned a1) { s16x4 lo = ldtr(a0), hi = ldtr(a1); return __builtin_shufflevector(lo, hi, 0, 1, 2, 3, 4, 5, 6, 7); }
DI void lds_flag(unsigned a, unsigned v) { asm volatile("s_mov_b64 exec, 1\n\tds_write_b32 %0, %1\n\ts_mov_b64 exec, -1" ::"v"(a), "v"(v) : "memory"); }
#define RETRY(CMP, RD, SLEEP) CMP "\n\ts_cbranch_vccz 2f\n\ts_mov_b32 %1, %4\n3:\n\t" SLEEP RD "\n\ts_waitcnt lgkmcnt(0)\n\t" CMP \
    "\n\ts_cbranch_vccz 2f\n\ts_sub_u32 %1, %1, 1\n\ts_cbranch_scc0 3b\n2:"
template <int NEED> DI void lds_chk(unsigned v, unsigned a) {
    unsigned c;
    asm volatile(RETRY("v_cmp_gt_u32 vcc, %3, %0", "ds_read_b32 %0, %2", "") : "+v"(v), "=&s"(c) : "v"(a), "i"(NEED), "i"(SPIN_LDS) : "vcc", "scc", "memory");
}
template <int NEED, int SLP = 0> DI void lds_wait(unsigned a) {
    unsigned v, c;
    asm volatile("s_mov_b32 %1, %4\n"
                 "1:\n\tds_read_b32 %0, %2\n\ts_waitcnt lgkmcnt(0)\n\tv_cmp_gt_u32 vcc, %3, %0\n\ts_cbranch_vccz 2f\n\ts_sleep %5\n\t"
                 "s_sub_u32 %1, %1, 1\n\ts_cbranch_scc0 1b\n2:"
                 : "=&v"(v), "=&s"(c) : "v"(a), "i"(NEED), "i"(SPIN_LDS), "i"(SLP) : "vcc", "scc", "memory");
}
template <int N, int SLP = 0> DI void land_wait(unsigned a) {
    unsigned v, c;
    asm volatile("s_mov_b32 %1, %4\n"
                 "1:\n\tds_read_b32 %0, %2 offset:%3\n\ts_waitcnt lgkmcnt(0)\n\tv_cmp_ne_u32 vcc, 0, %0\n\ts_cbranch_vccz 2f\n\t"
                 ".if %5\n\ts_sleep %5\n\t.endif\n\ts_sub_u32 %1, %1, 1\n\ts_cbranch_scc0 1b\n2:"
                 : "=&v"(v), "=&s"(c) : "v"(a), "i"(4 * N), "i"(SPIN_LDS), "i"(SLP) : "vcc", "scc", "memory");
}
template <int N, int SLP = 0> DI void land_chk(unsigned v, unsigned a) {
    unsigned c;
    asm volatile(RETRY("v_cmp_ne_u32 vcc, 0, %0", "ds_read_b32 %0, %2 offset:%3", ".if %5\n\ts_sleep %5\n\t.endif\n\t")
                 : "+v"(v), "=&s"(c) : "v"(a), "i"(4 * N), "i"(SPIN_LDS), "i"(SLP) : "vcc", "scc", "memory");
}
template <int OFF> DI void val_chk(float& d, unsigned a) {
    unsigned c;
    asm volatile(RETRY("v_cmp_eq_u32 vcc, -1, %0", "ds_read_b32 %0, %2 offset:%3", "") : "+v"(d), "=&s"(c) : "v"(a), "i"(OFF), "i"(SPIN_LDS) : "vcc", "scc", "memory");
}
DI void st3_wait(f32x2& a0, f32x2& a1, f32x2& a2, unsigned a) {
#pragma unroll 1
    for (unsigned c = 0; c < SPIN_LDS; ++c) {
        asm volatile("ds_read_b64 %0, %3\n\tds_read_b64 %1, %3 offset:512\n\tds_read_b64 %2, %3 offset:1024\n\ts_waitcnt lgkmcnt(0)" : "=&v"(a0), "=&v"(a1), "=&v"(a2) : "v"(a) : "memory");
        if (!__any(max(max(__float_as_uint(a0.y), __float_as_uint(a1.y)), __float_as_uint(a2.y)) == ~0u)) break;
    }
}
DI void bar_lds() { asm volatile("s_waitcnt lgkmcnt(0)\n\ts_barrier" ::: "memory"); }
template <class T> DI T ldsv(unsigned a) { return *(volatile LAS T*)(size_t)a; }
template <int SLP> DI unsigned lds_get(unsigned a) { lds_wait<1, SLP>(a); return rfl(ldsv<unsigned>(a + 4)); }
DI void lds_put(unsigned a, unsigned v) { lds_flag(a + 4, v); lds_flag(a, 1); }
template <int SLP = 1> DI void poll_flags(const char* ws, unsigned off, unsigned tag) {
    unsigned v, c;
    asm volatile("s_mov_b32 %1, %5\n"
                 "1:\n\tglobal_load_dword %0, %2, %3 sc1\n\ts_waitcnt vmcnt(0)\n\tv_cmp_ne_u32 vcc, %4, %0\n\ts_cbranch_vccz 2f\n\ts_sleep %6\n\t"
                 "s_sub_u32 %1, %1, 1\n\ts_cbranch_scc0 1b\n2:"
                 : "=&v"(v), "=&s"(c) : "v"(off), "s"(ws), "s"(tag), "i"(SPIN_GLB), "i"(SLP) : "vcc", "scc", "memory");
}
DI void drain() { asm volatile("s_waitcnt vmcnt(0)" ::: "memory"); }
DI void put_flag(char* ws, unsigned off, unsigned tag) { __hip_atomic_store((gu32*)(ws + off), tag, RLX_AGENT); }
DI void put_gran(char* ws, unsigned off, unsigned tag, unsigned val) { __hip_atomic_store((gu64*)(ws + off), ((u64)tag << 32) | val, RLX_AGENT); }
template <int N, int SLP = 2> DI void sweep(const char* ws, unsigned off, unsigned stride, unsigned tag, unsigned (&v)[N]) {
    for (unsigned s = 0; s < SPIN_GLB; ++s) {
        bool ok = true;
#pragma unroll
        for (int k = 0; k < N; ++k) { u64 x = __hip_atomic_load((gu64*)(ws + off + k * stride), RLX_AGENT); v[k] = (unsigned)x; ok &= (unsigned)(x >> 32) == tag; }
        if (__all(ok)) break;
        __builtin_amdgcn_s_sleep(SLP);
    }
}
DI i32x4 mk_srd(const void* p, unsigned bytes) { u64 a = (u64)p; return (i32x4){(int)(unsigned)a, (int)((unsigned)(a >> 32) & 0xffffu), (int)bytes, 0x00020000}; }
DI void dma(i32x4 srd, unsigned voff, unsigned soff, unsigned ldsdst) {
    asm volatile("s_mov_b32 m0, %3\n\ts_nop 0\n\tbuffer_load_dwordx4 %0, %1, %2 offen lds" ::"v"(voff), "s"(srd), "s"(soff), "s"(ldsdst) : "memory");
}
DI void dma4(i32x4 srd, const unsigned (&v)[4], unsigned soff, unsigned ldsdst) {
    asm volatile("s_mov_b32 m0, %6\n\ts_nop 0\n\tbuffer_load_dwordx4 %0, %4, %5 offen lds\n\tbuffer_load_dwordx4 %1, %4, %5 offen offset:1024 lds\n\t"
                 "buffer_load_dwordx4 %2, %4, %5 offen offset:2048 lds\n\tbuffer_load_dwordx4 %3, %4, %5 offen offset:3072 lds"
                 ::"v"(v[0]), "v"(v[1]), "v"(v[2]), "v"(v[3]), "s"(srd), "s"(soff), "s"(ldsdst) : "memory");
}
DI void dma_zero(i32x4 srd0, unsigned ldsdst) {
    asm volatile("s_mov_b32 m0, %1\n\ts_mov_b64 exec, 1\n\tbuffer_load_dword off, %0, 0 lds\n\ts_mov_b64 exec, -1" ::"s"(srd0), "s"(ldsdst) : "memory");
}
DI void touch(const void* base, unsigned voff) {
    asm volatile("s_mov_b32 m0, %2\n\ts_nop 0\n\tglobal_load_lds_dword %0, %1" ::"v"(voff), "s"(base), "s"(L_JUNK) : "memory");
}
DI void touch_ws(i32x4 srd, unsigned l16, unsigned soff, u64 mask) {
    asm volatile("s_mov_b32 m0, %3\n\ts_mov_b64 exec, %4\n\tbuffer_load_dword %0, %1, %2 offen lds\n\ts_mov_b64 exec, -1" ::"v"(l16 << 2), "s"(srd), "s"(soff), "i"(L_JUNK), "s"(mask) : "memory");
}
template <int N> DI void wait_vm() { asm volatile("s_waitcnt vmcnt(%0)" ::"i"(N) : "memory"); }
DI bf16x8 asfrag(u32x4 v) { return __builtin_bit_cast(bf16x8, v); }
DI u32x4 asu4(bf16x8 v) { return __builtin_bit_cast(u32x4, v); }
DI u32x4 pack8(const f32x16& a, int o) { return (u32x4){pk2(a[o], a[o + 1]), pk2(a[o + 2], a[o + 3]), pk2(a[o + 4], a[o + 5]), pk2(a[o + 6], a[o + 7])}; }
#define BLOAD(rs, vo, so, aux) __builtin_bit_cast(u32x4, __builtin_amdgcn_raw_buffer_load_b128((rs), (vo), (so), (aux)))
#define BSTORE(v, rs, vo, so, aux) do { const u32x4 d_ = (v); __builtin_amdgcn_raw_buffer_store_b128(d_, (rs), (vo), (so), (aux)); asm volatile("s_nop 1" ::"v"(d_) : "memory"); } while (0)
#define BSTORE2(v0, v1, rs, vo, so0, so1, aux) do { const u32x4 d0_ = (v0), d1_ = (v1); __builtin_amdgcn_raw_buffer_store_b128(d0_, (rs), (vo), (so0), (aux)); \
    __builtin_amdgcn_raw_buffer_store_b128(d1_, (rs), (vo), (so1), (aux)); asm volatile("s_nop 1" ::"v"(d0_), "v"(d1_) : "memory"); } while (0)
#define PUTLINE(val, off) do { unsigned l0_ = l16; PIN(l0_); unsigned tg_ = (val); asm volatile("" : "+s"(tg_)); \
    __builtin_amdgcn_raw_buffer_store_b32(tg_, rs_ws, FULLINE ? (l0_ >> 2) | ((l0_ >> 9) << 24) : l0_ << 20, (off), SC1); } while (0)
#define PUTFLAG(off) PUTLINE(tag, off)
#define FULLINE 1
#define PREW 5
#define PWPOS 9
constexpr int SC1 = 16;
constexpr unsigned OOB = 1u << 30;

#define STAMP(i) do { } while (0)
#define SSTAMP(i) do { } while (0)

constexpr int NPOS = 16;
#define ORD 0
constexpr int PK1 = ORD ? 3 : 4, PX2 = ORD ? 4 : 3;
constexpr int P_KIND[NPOS] = {0, 1, 0, ORD, 1 - ORD, 0, 1, 1, 2, 2, 2, 2, 3, 3, 3, 3};
constexpr int P_CHUNK[NPOS] = {0, 0, 1, 2 - ORD, 1 + ORD, 3, 2, 3, 0, 1, 2, 3, 0, 1, 2, 3};
constexpr int P_SLOT[NPOS] = {0, 4, 1, ORD ? 5 : 2, ORD ? 2 : 5, 3, 6, 7, 8, 0, 4, 1, 5, 2, 6, 7};
#define JX 0
#define PF 13

__global__ __launch_bounds__(512, 2) void fused_block(const u16*, const u16*, const u16*, char*, const u16*, const u16*, const u16*, const u16*, u16*) {
    typedef u64 u64x8 __attribute__((ext_vector_type(8)));
    u64x8 ka; u64 kb;
    const int tid = threadIdx.x, b = blockIdx.x;
    {
        u64 pc0; unsigned sa, va;
        asm volatile("s_load_dwordx16 %0, %5, 0x0\n\ts_load_dwordx2 %1, %5, 0x40\n\ts_getpc_b64 %2\n.Lfb_code0:\n\ts_andn2_b32 %3, %6, 7\n\ts_lshl_b32 %3, %3, 5\n\t"
                     "s_mov_b32 m0, %8\n\tv_lshl_add_u32 %4, %7, 7, %3\n\tv_min_u32 %4, .Lfb_code3-.Lfb_code0-64, %4\n\ts_mov_b64 exec, 3\n\tglobal_load_lds_dword %4, %2\n\tglobal_load_lds_dword %4, %2 offset:64\n\ts_mov_b64 exec, -1"
                     : "=&s"(ka), "=&s"(kb), "=&s"(pc0), "=&s"(sa), "=&v"(va) : "s"(__builtin_amdgcn_kernarg_segment_ptr()), "s"(b), "v"(tid), "i"(L_JUNK) : "scc", "memory");
    }
    u64 ts_[6] = {};
    SBAR();
    const unsigned wb = rfl(tid);
#define MAP 1
    const int p = MAP == 1 ? ((b & 6) << 2) | (b >> 5) : MAP == 0 ? ((b & 7) << 2) | (b >> 6) : ((b & 4) << 2) | (b >> 4);
    const int j = MAP == 1 ? ((b & 1) << 2) | ((b >> 3) & 3) : MAP == 0 ? (b >> 3) & 7 : ((b & 3) << 1) | ((b >> 3) & 1);
#define ROT 0
    const int rot = ROT == 0 ? (p + j) & 3 : ROT == 1 ? (((p + j) & 1) << 1) | ((b + (b >> 1)) & 1) : ROT == 2 ? ((b >> 1) + 2 * b) & 3 : ((p + j) & 1) << 1;
#define R0(base, ld) asm volatile("s_mov_b32 m0, %5\n\ts_waitcnt lgkmcnt(0)\n\tglobal_load_lds_dwordx4 %0, %4\n\tglobal_load_lds_dwordx4 %1, %4 offset:1024\n\t" \
        "global_load_lds_dwordx4 %2, %4 offset:2048\n\tglobal_load_lds_dwordx4 %3, %4 offset:3072" ::"v"(v0), "v"(v1), "v"(v2), "v"(v3), "s"(base), "s"(ld) : "memory")
    {
        if (wb < 256) {
            const unsigned v0 = p * 65536 + rot * 256 + ((tid << 9) & 0x8000) + (tid & 128) + ((tid << 7) & 0x1c00) + (((tid ^ ((tid >> 4) & 3)) & 7) << 4), v1 = (v0 ^ 64) + 7168, v2 = v0 + 14336, v3 = v1 + 14336;
            R0(ka[0], wb << 6);
        } else {
            const unsigned v0 = (rot * 128 + ((tid >> 1) & 96) + ((tid >> 3) & 7)) * 3072 + 1024 + j * 128 + (((tid ^ ((tid >> 2) & 4)) & 7) << 4), v1 = v0 + 23552, v2 = v0 + 2 * 23552, v3 = v0 + 3 * 23552;
            R0(ka[1], (wb << 6) + 49152);
        }
    }
    SBAR();
    asm volatile("" : "+s"(ka), "+s"(kb));
    const u16 *x = (const u16*)ka[0], *wqkv = (const u16*)ka[1], *wr = (const u16*)ka[2], *wo = (const u16*)ka[4], *wg = (const u16*)ka[5], *wu = (const u16*)ka[6], *wd = (const u16*)ka[7];
    char* ws = (char*)ka[3];
    const int lane = tid & 63, wid = wb >> 6, blk = p * 8 + j;
    STAMP(16);
    {
        const unsigned iv = wid < 3 ? 0x3f803f80u : wid == 3 ? 0u : ~0u;
        if (wid < 7) ldsw<u32x4>(L_MISC + tid * 16, (u32x4){iv, iv, iv, iv});
        else asm volatile("s_mov_b32 m0, %2\n\ts_nop 0\n\tglobal_load_lds_dwordx4 %0, %1" ::"v"(((lane + 16 * rot) & 63) * 16), "s"(wr), "i"(L_WR) : "memory");
    }
    bar_lds();
    const int r8 = lane >> 3, ph = lane & 7;
    const unsigned swz = (ph ^ (((r8 >> 1) & 1) << 2)) * 16;
    const unsigned vx0 = r8 * 1024 + ((ph ^ (r8 >> 1)) * 16), vw0 = r8 * 3072 + swz;
    const i32x4 s0 = mk_srd(x, 0);
    dma_zero(s0, L_LAND + 64 + 4 * wid);
    unsigned ep, tag;
    asm volatile("s_load_dword %0, %1, 0x0" : "=s"(ep) : "s"(ws) : "memory");
    const int l31 = lane & 31, h = lane >> 5;
    const auto rs_ws = __builtin_amdgcn_make_buffer_rsrc(ws, 0, WS_USED, 0x00020000);
    const auto rs_x = __builtin_amdgcn_make_buffer_rsrc((void*)x, 0, S_ * D_ * 2, 0x00020000);
    const auto rs_out = __builtin_amdgcn_make_buffer_rsrc((void*)(((u64)rfl((unsigned)(kb >> 32)) << 32) | rfl((unsigned)kb)), 0, S_ * D_ * 2, 0x00020000);
    unsigned obase = p * 65536 + j * 128;
    asm volatile("" : "+s"(obase));
    u32x4 xchunk;
    f32x4 za, zb;
    unsigned e_pa, e_pb, e_oo, e_row;
#define EPI_PREP() do { const int tok_ = (tid >> 3) ^ 32, cg_ = tid & 7; \
        const float mb_ = ldsr<float>(L_MEAN + tok_ * 4), rs_ = ldsr<float>(L_RSTD + tok_ * 4); \
        _Pragma("unroll") for (int d = 0; d < 4; ++d) { const unsigned d2 = pk2(blo(xchunk[d]) - mb_, bhi(xchunk[d]) - mb_), n2 = pk2(blo(d2) * rs_, bhi(d2) * rs_); \
            (d < 2 ? za : zb)[2 * (d & 1)] = blo(xchunk[d]) + blo(n2); (d < 2 ? za : zb)[2 * (d & 1) + 1] = bhi(xchunk[d]) + bhi(n2); } \
        e_pa = (tok_ & 31) * 272 + cg_ * 32; e_pb = e_pa + (wid >= 4 ? L_PB : L_VREM + 2 * VREM_STRIDE); e_pa += wid >= 4 ? L_PA : L_VREM; e_oo = tok_ * 1024 + cg_ * 16; e_row = 2047 - (64 * p + tok_); \
        asm volatile("" : "+v"(za), "+v"(zb), "+v"(e_pa), "+v"(e_pb), "+v"(e_oo), "+v"(e_row)); } while (0)
#define OP_PREP(qt, kq, pbase) int ln = tid; PIN(ln); \
        unsigned ob0 = L_WO + (kq) * 16384 + (((ln >> 3) & 4) + ((ln >> 2) & 3)) * 128 + (((((ln >> 3) & 2) + ((ln >> 1) & 1)) ^ ((ln >> 1) & 4)) * 16) + 8 * (ln & 1), ob1 = ob0 ^ 64; \
        unsigned fo2 = WS_FK + (p * 32 + 8 * (kq) + (ln & 7)) * 512 + 128 + 128 * (qt), pa = (pbase) + (ln & 31) * 272 + ((ln >> 1) & 16), ol = (ln & 63) * 16; \
        unsigned so = WS_O + (unsigned)(p * 64 + 32 * (qt) + 8 * (kq)) * 1024u, so1 = so + 4096; \
        PIN(ob0); PIN(ob1); PIN(fo2); PIN(pa); PIN(ol); asm volatile("" : "+s"(so), "+s"(so1));
#define OP_WO() lds_wait<1>(W_WORDY + 4 * (ln & 1)); bf16x8 w0[8], w1[8]; \
        _Pragma("unroll") for (int i = 0; i < 8; ++i) { w0[i] = ldtr2(ob0 + i * 2048, ob0 + i * 2048 + 1024); w1[i] = ldtr2(ob1 + i * 2048, ob1 + i * 2048 + 1024); } SBAR();
#define O0(i) do { c0 = MFMA32(w0[i], of[i], c0); PIN(c0); SBAR(); } while (0)
#define O1(i) do { c1 = MFMA32(w1[i], of[i], c1); PIN(c1); SBAR(); } while (0)
#define OW(c, o, g) do { ldsw<f32x4>(pa + (o) + (g) * 32, (f32x4){c[4 * (g)], c[4 * (g) + 1], c[4 * (g) + 2], c[4 * (g) + 3]}); SBAR(); } while (0)
#define OP_RUN(EXTRA) do { bf16x8 of[8]; f32x16 c0 = {}, c1 = {}; \
        _Pragma("unroll") for (int i = 0; i < 8; ++i) of[i] = asfrag(BLOAD(rs_ws, ol + (i & 3) * 1024, i < 4 ? so : so1, SC1)); SBAR(); EXTRA; SBAR(); \
        O0(0); O1(0); O0(1); O1(1); O0(2); O1(2); O0(3); O0(4); O0(5); O0(6); O0(7); O1(3); O1(4); O1(5); \
        OW(c0, 0, 0); OW(c0, 0, 1); O1(6); OW(c0, 0, 2); OW(c0, 0, 3); O1(7); OW(c1, 128, 0); OW(c1, 128, 1); OW(c1, 128, 2); OW(c1, 128, 3); } while (0)
#define RPF 0
#define WOLAG 8
#define OSLP 1
#define ODLY 8
#define ODLY0 12
#define TAG() asm volatile("s_waitcnt lgkmcnt(0)\n\ts_add_u32 %0, %1, 1" : "=s"(tag) : "s"(ep) : "scc", "memory")

    if (__builtin_expect(wid >= 4, 1)) {
        const int lw = wid - 4;
#define SPRIO 2
#define SSLP 1
        __builtin_amdgcn_s_setprio(SPRIO);
#define MAUX 2
#define VDIR_ 0
#define W2E 99
#define YW 0
#define SPREAD 0
#define W2P 2
#define TRE 0
        int jslow = 0, n2 = 0, cg = 0, kg = 0;
        bool st1 = false, st2 = false;
        const int sv = lw - 2;
#define ROLE() do { jslow = (3 - (int)rfl((unsigned)((u64)wqkv >> 7))) & 7; const int n1 = (SPREAD ? (p & 7) * 4 + (p >> 3) : p) * 7 + j - (j > jslow); n2 = n1 - 176; kg = n1 / 22; cg = n1 - 22 * kg; \
        st2 = j != jslow && (unsigned)n2 < 43u; st1 = j != jslow && n2 < 0; } while (0)
        u32x4 wm[16];
#define LOAD_GU() do { const auto rs_ = __builtin_amdgcn_make_buffer_rsrc((void*)(sv ? wu : wg), 0, D_ * F_ * 2, 0x00020000); \
        const unsigned vo_ = (lane >> 4) * (8 * F_ * 4) + (lane & 15) * 16 + (cg == 21 ? (lane & 8) << 24 : 0); \
        _Pragma("unroll") for (int i = 0; i < 8; ++i) wm[i] = BLOAD(rs_, vo_, (32 * kg + i) * (F_ * 4) + cg * 256, MAUX); SBAR(); } while (0)
#define LOAD_D() do { const auto rs_ = __builtin_amdgcn_make_buffer_rsrc((void*)wd, 0, D_ * F_ * 2, 0x00020000); \
        _Pragma("unroll") for (int i = 0; i < 16; ++i) wm[i] = BLOAD(rs_, (lane >> 4) * 8192 + (lane & 15) * 16 + (i & 3) * 1024 + (i >> 3) * 256, n2 * 32768 + sv * 512 + (i & 4) * 1024, MAUX); SBAR(); } while (0)
#define TR8(o) do { u32x4 t_[8]; _Pragma("unroll") for (int e = 0; e < 8; ++e) _Pragma("unroll") for (int i = 0; i < 4; ++i) \
            t_[e][i] = __builtin_amdgcn_perm(wm[(o) + 2 * i + 1][e >> 1], wm[(o) + 2 * i][e >> 1], (e & 1) ? 0x07060302u : 0x05040100u); \
        _Pragma("unroll") for (int e = 0; e < 8; ++e) { wm[(o) + e] = t_[e]; PIN(wm[(o) + e]); } } while (0)
#define GEMV(o, A, off, ODD) do { float d_[8]; _Pragma("unroll") for (int e = 0; e < 8; ++e) { const f32x4 r_ = MFMA16(asfrag(A), asfrag(wm[(o) + e]), (f32x4){}); d_[e] = (ODD) ? r_[1] : r_[0]; } \
        const float e0_ = sel4(LM(l & 16), LM(l & 32), d_[0], d_[2], d_[4], d_[6]), e1_ = sel4(LM(l & 16), LM(l & 32), d_[1], d_[3], d_[5], d_[7]); \
        BSTORE(((u32x4){__float_as_uint(e0_), tag, __float_as_uint(e1_), tag}), rs_ws, (lane & 15) * 64 + (lane >> 4) * 16, (off), SC1); } while (0)
        unsigned a_freeall = W_FREE + 4 * (lane & 3), a_full = L_LAND;
        PIN(a_freeall); PIN(a_full);
        if (lw == 0) {
            const i32x4 sx = mk_srd(x, S_ * D_ * 2), sw = mk_srd(wqkv, D_ * 3 * D_ * 2);
            const unsigned vx[4] = {vx0, (vx0 ^ 64) + 7168, vx0 + 2 * 7168, (vx0 ^ 64) + 3 * 7168};
            const unsigned vw[4] = {vw0, vw0 + 23552, vw0 + 2 * 23552, vw0 + 3 * 23552};
            const unsigned prow = (MAP == 0 ? (p & 3) * 128 : MAP == 1 ? (p & 7) * 64 : (p & 15) * 32) + (lane >> 1), pc = j * 128 + (lane & 1) * 64;
            unsigned freev, a_mark = L_LAND + 4 * ((lane & 7) == 4 ? 0 : lane & 7);
            PIN(a_mark);
#define PL 0
#define LAG 40
#define TPOS 8
#define OPOS 15
#define GRP(n, g) do { const unsigned kc_ = (P_CHUNK[n] + rot) & 3; if ((n) > 2) wait_vm<LAG>(); \
            if (P_KIND[n] == 0) dma4(sx, vx, p * 65536 + kc_ * 256 + ((g) >> 1) * 128 + ((g) & 1) * 32768, P_SLOT[n] * SLOT + (g) * 4096); \
            else dma4(sw, vw, j * 128 + kc_ * (128 * 3072) + (g) * (32 * 3072) + (P_KIND[n] % 3) * 1024, P_SLOT[n] * SLOT + (g) * 4096); } while (0)
#define TOUCH(n) do { _Pragma("unroll") for (int i = 0; i < (4 >> MAP); ++i) { if ((PF & 8) && (n) == TPOS) touch(wqkv, (prow + 32 * i) * 3072 + pc); \
            if ((PF & 4) && (n) == OPOS) touch(wo, (prow + 32 * i) * 1024 + pc); } } while (0)
#define ISSUE(n) do { if ((n) == 9) land_chk<24>(freev, a_mark); if ((n) == 11) land_chk<32>(freev, a_mark); if ((n) == 14) land_chk<40>(freev, a_mark); \
            GRP(n, 0); GRP(n, 1); GRP(n, 2); if ((n) == 8 || (n) == 10 || (n) == 13) freev = ldsv<unsigned>(a_mark + ((n) == 8 ? 96 : (n) == 10 ? 128 : 160)); GRP(n, 3); dma_zero(s0, L_LAND + 4 * (n)); TOUCH(n); if ((n) >= 6) STAMP(32 + (((n) + 4) % 10)); } while (0)
            TOUCH(1);
            ISSUE(2); ISSUE(3); ISSUE(4); ISSUE(5); ISSUE(6); ISSUE(7); ISSUE(8); TAG(); __builtin_amdgcn_s_setprio(PL); ISSUE(9); ISSUE(10); ISSUE(11); ISSUE(12); ISSUE(13); ISSUE(14); ISSUE(15);
            xchunk = BLOAD(rs_x, ((tid >> 3) & 31) * 1024 + (tid & 7) * 16, obase, 0);
        } else {
            ROLE();
#define GUP 1
#define LOAD_M() do { if (lw >= 2 && st1) LOAD_GU(); if (W2E == 99 && lw >= 2 && st2) LOAD_D(); } while (0)
            if (GUP == 1) LOAD_M();
            const int row = lane & 15, g = lane >> 4, hs = lw - 1;
            const unsigned xb0 = (row >> 3) * 1024 + (row & 7) * 128 + ((g ^ (row >> 1)) * 16), ab0 = ((row & 3) == 1 ? L_WR : L_ONES + 64) + g * 16;
            unsigned xa = (xb0 ^ ((hs & 1) * 64)) + (hs >> 1) * 8192, xe = (xb0 ^ 64) + 8192, aa = ab0 + hs * 64, ae = ab0 + 192, a_r0 = L_LAND + 4 * (lane & 7), fv;
            f32x4 sm[4] = {}, gr[4] = {};
            bf16x8 y[5], z[5];
            PIN(xa); PIN(xe); PIN(aa); PIN(ae); PIN(a_r0);
#define ST_RD(Y, XA, AA, c) do { Y[0] = ldsr<bf16x8>(XA + (c) * SLOT); Y[4] = ldsr<bf16x8>(AA + (c) * 256); Y[1] = ldsr<bf16x8>(XA + (c) * SLOT + 2048); \
            Y[2] = ldsr<bf16x8>(XA + (c) * SLOT + 4096); Y[3] = ldsr<bf16x8>(XA + (c) * SLOT + 6144); SBAR(); } while (0)
#define ST_MM(Y) do { _Pragma("unroll") for (int u = 0; u < 4; ++u) { gr[u] = MFMA16(Y[u], Y[u], gr[u]); sm[u] = MFMA16(Y[4], Y[u], sm[u]); asm volatile("" : "+v"(gr[u]), "+v"(sm[u])); SBAR(); } } while (0)
#define ST_CH(c, nxt) do { ST_RD(y, xa, aa, c); if (hs == (c) % 3) { ST_RD(z, xe, ae, c); ST_MM(y); if (nxt) fv = ldsv<unsigned>(a_full + 4 * (nxt)); SBAR(); ST_MM(z); } \
            else { if (nxt) fv = ldsv<unsigned>(a_full + 4 * (nxt)); SBAR(); ST_MM(y); } \
            if (TRE && (c) == 0 && lw >= 2 && st1) TR8(0);     \
            if (!((c) & 1)) dma_zero(s0, L_LAND + 96 + 16 * (c) + 4 * wid); if (nxt) land_chk<nxt, SSLP>(fv, a_full); } while (0)
            TAG();
            if ((PREW & 1) && lw == 1) { const unsigned l16 = lane * 16; PUTLINE(~tag, WS_FV + blk * 512); }
            if (lw == 1 && j == jslow) touch(ws, WS_RG + p * RGS);
            if ((PREW & 2) && lw >= 2) {
                const u32x4 nt = {~tag, ~tag, ~tag, ~tag};
                if (st1) BSTORE(nt, rs_ws, lane * 16, WS_G1 + ((sv * 8 + kg) * 2816 + cg * 128) * 8, SC1);
                if (st2) BSTORE2(nt, nt, rs_ws, lane * 16, WS_G2 + n2 * 4096 + sv * 2048, WS_G2 + n2 * 4096 + sv * 2048 + 1024, SC1);
            }
            SSTAMP(16 + (1)); land_wait<16, SSLP>(a_r0); SSTAMP(16 + (2));
            if (GUP == 2) LOAD_M();
            float sw_ = 0.f;
            if (lw == 2 && j == jslow) {
                const u32x4 w4 = ldsr<u32x4>(L_WR + lane * 16);
                sw_ = wave_sum(dot2(w4.x, 0x3f803f80u, dot2(w4.y, 0x3f803f80u, dot2(w4.z, 0x3f803f80u, dot2(w4.w, 0x3f803f80u, 0.f)))));
            }
            PIN(sw_);
            ST_CH(0, 2); ST_CH(1, PX2); if (GUP == 4) LOAD_M(); ST_CH(2, 5); if (GUP == 5) LOAD_M(); ST_CH(3, 0);
            SSTAMP(16 + (3));
            {
                const int u = g ^ (row >> 2);
                const bool e0 = LM(l & 1), e1 = LM(l & 2), u0 = LM(((l >> 4) ^ (l >> 2)) & 1), u1 = LM(((l >> 4) ^ (l >> 2)) & 2), g0 = LM(l & 16), g1 = LM(l & 32);
                unsigned d[4];
#pragma unroll
                for (int q = 0; q < 4; ++q) d[q] = __float_as_uint(sel4(e0, e1, gr[q][0], gr[q][1], gr[q][2], gr[q][3]));
                const auto t2 = __builtin_amdgcn_permlane32_swap(d[2], d[2], false, false), t3 = __builtin_amdgcn_permlane32_swap(d[3], d[3], false, false);
                const unsigned ev = u1 ? (g1 ? t2[0] : t2[1]) : d[0], od = u1 ? (g1 ? t3[0] : t3[1]) : d[1];
                const auto t1 = __builtin_amdgcn_permlane16_swap(od, od, false, false);
                const unsigned ssq = u0 ? (g0 ? t1[0] : t1[1]) : ev;
                ldsw<f32x2>(L_STP + hs * 512 + (16 * u + row) * 8, (f32x2){sel4(u0, u1, sm[0][0], sm[1][0], sm[2][0], sm[3][0]), __uint_as_float(ssq)});
                CFENCE();
                ldsw<float>(L_XWP + hs * 256 + (16 * u + row) * 4, sel4(u0, u1, sm[0][1], sm[1][1], sm[2][1], sm[3][1]));
                dma_zero(s0, L_LAND + 160 + 4 * wid);
                if (GUP == 6) LOAD_M();
            }
            if (lw == 2) {
                f32x2 a0, a1, a2;
                float x0, x1, x2;
#pragma unroll 1
                for (unsigned c = 0; c < SPIN_LDS; ++c) {
                    asm volatile("ds_read_b32 %3, %7\n\tds_read_b32 %4, %7 offset:256\n\tds_read_b32 %5, %7 offset:512\n\t"
                                 "ds_read_b64 %0, %6\n\tds_read_b64 %1, %6 offset:512\n\tds_read_b64 %2, %6 offset:1024\n\ts_waitcnt lgkmcnt(0)"
                                 : "=&v"(a0), "=&v"(a1), "=&v"(a2), "=&v"(x0), "=&v"(x1), "=&v"(x2) : "v"(L_STP + lane * 8), "v"(L_XWP + lane * 4) : "memory");
                    if (!__any(max(max(__float_as_uint(x0), __float_as_uint(x1)), __float_as_uint(x2)) == ~0u)) break;
                }
                const float mean = ((a0.x + a1.x) + a2.x) * (1.f / D_), var = fmaxf(((a0.y + a1.y) + a2.y) * (1.f / D_) - mean * mean, 0.f);
                const float mb = rbf(mean), sd = rbf(__builtin_amdgcn_sqrtf(rbf(rbf(var) + 1e-5f))), rstd = __builtin_amdgcn_rcpf(sd);
                if (j == jslow) {
                    const float rv = (((x0 + x1) + x2) - mb * sw_) * rstd;
                    const unsigned bits = pk2(rv, rv) & 0xffffu;
                    const unsigned kmax = wave_max(((bits ^ ((bits & 0x8000u) ? 0xffffu : 0x8000u)) << 16) | (unsigned)(2047 - (64 * p + lane)));
                    const unsigned ms = __builtin_amdgcn_readlane((int)pk2(mb, sd), (int)(~kmax & 63u));
                    if (lane < 2) put_gran(ws, WS_RG + p * RGS + lane * 8, tag, lane ? ms : kmax);
                    SSTAMP(38);
                }
                ldsw<float>(L_MEAN + lane * 4, mb); ldsw<float>(L_RSTD + lane * 4, rstd);
                lds_flag(W_STAT, 1);
            }
#define MOEPOS 11
#define YSLP 4
#define RSLP 12
#define P2 0
#define WS2 4
            __builtin_amdgcn_s_setprio(P2);
            xchunk = BLOAD(rs_x, ((tid >> 3) & 31) * 1024 + (tid & 7) * 16, obase, 0);
            if (lw >= 2) {
                if (!TRE && st1) TR8(0);
                if (W2E == 99 && st2) { TR8(0); TR8(8); }
                if (st1) land_wait<MOEPOS, WS2>(a_full);
                else { lds_wait<1, WS2>(W_STAT); EPI_PREP(); }
            }
        }
        tag = rfl(tag);
        bool ytodo = false;
#define Y_LD() do { _Pragma("unroll") for (int k = 0; k < 11; ++k) gy[k] = BLOAD(rs_ws, h * 4096 + l31 * 16, WS_G2 + sv * 90112 + k * 8192 + j * 512, SC1); } while (0)
#define Y_OK() ({ if (sv && h) gy[10] = (u32x4){0u, tag, 0u, tag}; unsigned all1 = ~0u, any1 = 0; ys = (f32x2){0.f, 0.f}; \
            _Pragma("unroll") for (int k = 0; k < 11; ++k) { all1 &= gy[k].y & gy[k].w; any1 |= gy[k].y | gy[k].w; ys.x += __uint_as_float(gy[k].x); ys.y += __uint_as_float(gy[k].z); } \
            __all(all1 == tag && any1 == tag); })
        SSTAMP(1);
        if (lw < 2) {
#define WOE 0
            if (WOE == 2 && lw == 0) ROLE();
            const i32x4 so = mk_srd(wo, D_ * D_ * 2);
            const unsigned vo0 = r8 * 1024 + swz, vo[4] = {vo0, vo0 + 7168, vo0 + 2 * 7168, vo0 + 3 * 7168};
#define WO_LD(hf) do { _Pragma("unroll") for (int c = 0; c < 8; ++c) { const unsigned kc = (c + p) & 7; if (WOLAG < 32) wait_vm<WOLAG>(); \
                dma4(so, vo, (kc * 64 + (hf) * 32) * 1024 + j * 128, L_WO + kc * 8192 + (hf) * 4096); } } while (0)
            if (lw == 1) {
                land_wait<PWPOS, WS2>(a_full);
                if (PREW & 8) { const unsigned l16 = lane * 16; PUTLINE(~tag, WS_FV + blk * 512); }
                land_wait<13, WS2>(a_full);
            }
            __builtin_amdgcn_s_setprio(2);
            if (lw == 0) {
#define KGATE 5
#define KSLP 1
                if (KGATE) { lds_wait<1, 2>(W_VOWN); __builtin_amdgcn_s_sleep(KGATE - 1); }
                if (p) poll_flags<KSLP>(ws, WS_FK + ((max(p - 4 + ((lane >> 2) & 3), 0) * 8 + j) * 4 + (lane & 3)) * 512, tag);
                SSTAMP(22);
                lds_flag(W_KSEEN, 1);
            }
            if (lw == 1) {
                SSTAMP(29);
                const unsigned l16 = lane * 16, gv = WS_V + (unsigned)(p * 32 + 4 * j) * 2048u;
                if (VDIR_) { lds_wait<1, 2>(W_VOWN + 4 * (lane & 3)); drain(); lds_wait<2>(W_VOWN + 4 * (lane & 3)); }
#pragma unroll
                for (int u = 0; u < (VDIR_ ? 0 : 2); ++u) {
                    lds_wait<1, 1>(W_VOWN + 8 * u + 4 * (lane & 1));
                    const u32x4 a0 = ldsr<u32x4>(L_VOWN + u * 4096 + l16), a1 = ldsr<u32x4>(L_VOWN + u * 4096 + 1024 + l16);
                    const u32x4 a2 = ldsr<u32x4>(L_VOWN + u * 4096 + 2048 + l16), a3 = ldsr<u32x4>(L_VOWN + u * 4096 + 3072 + l16);
                    BSTORE2(a0, a1, rs_ws, l16, gv + u * 4096, gv + u * 4096 + 1024, SC1); BSTORE2(a2, a3, rs_ws, l16, gv + u * 4096 + 2048, gv + u * 4096 + 3072, SC1);
                }
                SSTAMP(45);
                drain();
                PUTFLAG(WS_FV + blk * 512); CFENCE();
                if (RPF & 2) { touch_ws(mk_srd(ws, WS_USED), l16, gv, ~0ull); touch_ws(mk_srd(ws, WS_USED), l16, gv + 4096, ~0ull); }
                SSTAMP(46);
            }
            lds_wait<1, 1>(a_freeall);
            {
#pragma unroll
                for (int i = 0; i < 4; ++i) { const unsigned o_ = p - 4 + 2 * lw + (i >> 1) >= 0 ? 0x3f803f80u : 0u; ldsw<u32x4>(L_ONES8 + lw * 4096 + i * 1024 + lane * 16, (u32x4){o_, o_, o_, o_}); }
                SSTAMP(31);
                lds_flag(W_ONES + 4 * lw, 1);
#define VDLY 3
#define VSLP 1
                if (lw == 0) {
                    if (VDLY) { lds_wait<1, 1>(W_ONES + 4); __builtin_amdgcn_s_sleep(VDLY - 1); }
                    if (p) poll_flags<VSLP>(ws, WS_FV + (max(p - 4 + (lane & 3), 0) * 8 + j) * 512, tag);
                    lds_flag(W_VSEEN, 1);
                }
                else { if (WOE == 1 || (WOE == 2 && st2)) { WO_LD(0); WO_LD(1); } lds_wait<1>(W_VSEEN); }
                SSTAMP(47);
                u32x4 vv[16];
#pragma unroll
                for (int i = 0; i < 16; ++i) vv[i] = BLOAD(rs_ws, lane * 16 + (i & 3) * 1024, p - 4 + (i >> 2) >= 0 ? WS_V + (unsigned)((p - 4 + (i >> 2)) * 32 + 4 * j + 2 * lw) * 2048u : OOB, SC1);
                lds_flag(W_VISS + 4 * lw, 1);
                lds_wait<1>(W_ONES + 4 * (lane & 1));
#pragma unroll
                for (int i = 0; i < 16; ++i) ldsw<u32x4>(L_VREM + (2 * lw + ((i >> 1) & 1)) * VREM_STRIDE + (2 * (i >> 2) + (i & 1)) * 1024 + lane * 16, vv[i]);
                SSTAMP(30);
                lds_flag(W_VRDY + 4 * lw, 1);
            }
            if (!(WOE == 1 || (WOE == 2 && st2))) WO_LD(lw);
            wait_vm<0>();
            lds_flag(W_WORDY + 4 * lw, 1);
            SSTAMP(4);
            __builtin_amdgcn_s_setprio(0);
            EPI_PREP();
        } else {
            unsigned key, ms;
#define ARGMAX() do { if (sv == 0) { unsigned kv_[1]; sweep<1, RSLP>(ws, WS_RG + l31 * RGS + h * 8, 0, tag, kv_); \
                key = wave_max(LM(l < 32) ? kv_[0] : 0u); ms = __builtin_amdgcn_readlane((int)kv_[0], (int)(32 + ((2047 - (key & 0xffffu)) >> 6))); \
                ldsw<u32x2>(W_KEY, (u32x2){key, ms}); lds_flag(W_KEYV, 1); \
            } else { lds_wait<1, 2>(W_KEYV); const u32x2 km_ = ldsv<u32x2>(W_KEY); key = rfl(km_.x); ms = rfl(km_.y); } } while (0)
#define PM 2
            __builtin_amdgcn_s_setprio(PM);
            if (!st1) land_wait<15, WS2>(a_full);
            ARGMAX();
            SSTAMP(37);
            if (st2) {
#define G1SLP 4
                if (W2E && W2E != 99) { lds_wait<1, 1>(W_ONES + 4); __builtin_amdgcn_s_sleep(W2E - 1); LOAD_D(); __builtin_amdgcn_s_setprio(W2P); TR8(0); TR8(8); __builtin_amdgcn_s_setprio(PM); }
#define G1E 8
                if (!G1E) lds_wait<1, 2>(W_VISS + 4 * (lane & 1));
                if (G1E >= 2) { lds_wait<1, 2>(W_ONES + 4); __builtin_amdgcn_s_sleep(G1E - 2); }
                SSTAMP(21);
                f32x2 s2;
                u32x2 gv[16];
#define G1_LD() do { _Pragma("unroll") for (int k = 0; k < 16; ++k) \
                    gv[k] = __builtin_bit_cast(u32x2, __builtin_amdgcn_raw_buffer_load_b64(rs_ws, (h * 8 * 2816 + l31) * 8, WS_G1 + ((k >> 1) * 2816 + (k & 1) * F_ + 32 * n2) * 8, SC1)); SBAR(); } while (0)
#define G1_OK() ({ unsigned all1 = ~0u, any1 = 0; s2 = (f32x2){0.f, 0.f}; _Pragma("unroll") for (int k = 0; k < 16; k += 2) { all1 &= gv[k].y & gv[k + 1].y; any1 |= gv[k].y | gv[k + 1].y; \
                    s2.x += __uint_as_float(gv[k].x); s2.y += __uint_as_float(gv[k + 1].x); } __all(all1 == tag && any1 == tag); })
                G1_LD();
                if (!W2E) LOAD_D();
#define PS2 2
                __builtin_amdgcn_s_setprio(PS2);
                if (__builtin_expect(!G1_OK(), 0)) {
#pragma unroll 1
                    for (unsigned c = 0; c < SPIN_GLB; ++c) {
                        poll_flags<G1SLP>(ws, WS_G1 + ((l31 >> 1) * 2816 + (l31 & 1) * F_ + 32 * n2) * 8 + 4, tag);
                        G1_LD();
                        if (G1_OK()) break;
                    }
                }
                SSTAMP(3);
                const float sum = rbf(s2.x + s2.y);
                auto sw2 = __builtin_amdgcn_permlane32_swap(__float_as_uint(sum), __float_as_uint(sum), false, false);
                const float g = __uint_as_float(sw2[0]), u = __uint_as_float(sw2[1]);
                const float hv = rbf(g * __builtin_amdgcn_rcpf(1.f + __expf(-g))) * u;
                const int hb = pk2(hv, DPPF(hv, 0xB1)), ba = (lane >> 4) * 32;
                const u32x4 ha = {(unsigned)__builtin_amdgcn_ds_bpermute(ba, hb), (unsigned)__builtin_amdgcn_ds_bpermute(ba + 8, hb),
                                  (unsigned)__builtin_amdgcn_ds_bpermute(ba + 16, hb), (unsigned)__builtin_amdgcn_ds_bpermute(ba + 24, hb)};
                if (!W2E) TR8(0);
                GEMV(0, ha, WS_G2 + n2 * 4096 + sv * 2048, false);
                SSTAMP(20);
                if (!W2E) TR8(8);
                GEMV(8, ha, WS_G2 + n2 * 4096 + sv * 2048 + 1024, false);
                SSTAMP(4);
            } else if (st1) {
                const unsigned xs = (2047 - (key & 0xffffu)) * 1024 + kg * 128, ps = (lane & 1) ? 0x07060302u : 0x05040100u;
                const u32x4 xa_ = BLOAD(rs_x, (lane >> 4) * 32, xs, 0), xb_ = BLOAD(rs_x, (lane >> 4) * 32 + 16, xs, 0);
                u32x4 xo = {__builtin_amdgcn_perm(xa_[1], xa_[0], ps), __builtin_amdgcn_perm(xa_[3], xa_[2], ps), __builtin_amdgcn_perm(xb_[1], xb_[0], ps), __builtin_amdgcn_perm(xb_[3], xb_[2], ps)};
                const float mb = blo(ms), rstd = __builtin_amdgcn_rcpf(bhi(ms));
#pragma unroll
                for (int i = 0; i < 4; ++i) { const unsigned d2 = pk2(blo(xo[i]) - mb, bhi(xo[i]) - mb); xo[i] = pk2(blo(d2) * rstd, bhi(d2) * rstd); }
                SSTAMP(2);
                const bool odd = cg * 128 + (lane & 15) * 8 >= F_;
                GEMV(0, xo, WS_G1 + ((sv * 8 + kg) * 2816 + cg * 128) * 8, odd);
                SSTAMP(4);
            }
            if (st1) { __builtin_amdgcn_s_setprio(0); lds_wait<1>(W_STAT); EPI_PREP(); __builtin_amdgcn_s_setprio(PM); }
            ytodo = ((2047 - (int)(key & 0xffffu)) >> 6) == p;
            SSTAMP(5);
            if (blk == 0 && sv == 1) {
                lds_wait<1, 2>(W_VRDY + 4 * (lane & 1));
#pragma unroll 1
                for (int i = 0; i < 4; ++i) poll_flags<8>(ws, WS_FV + (i * 64 + lane) * 512, tag);
                if (lane == 0) put_flag(ws, WS_EPOCH, tag);
            }
        }
        {
            __builtin_amdgcn_s_setprio(0);
            OP_PREP(0, lw, (lw < 2 ? L_PA : L_PB) + (lw & 1) * VREM_STRIDE);
            OP_WO();
            lds_wait<1, 2>(W_ODRN + 4 * (ln & 3));
#define PO 2
            __builtin_amdgcn_s_setprio(PO);
            __builtin_amdgcn_s_sleep(ODLY0);
            poll_flags<OSLP>(ws, fo2, tag);
            SSTAMP(26);
            if (__builtin_expect(ytodo, 0)) {
                u32x4 gy[11];
                f32x2 ys;
                OP_RUN(Y_LD());
                if (!Y_OK()) {
#pragma unroll 1
                    for (unsigned c = 0; c < SPIN_GLB; ++c) {
                        Y_LD();
                        if (Y_OK()) break;
                        poll_flags<1>(ws, WS_G2 + min(sv * 22 + min(lane, 21), 42) * 4096 + j * 512 + 4, tag);
                    }
                }
                ys.x = swap32add(ys.x); ys.y = swap32add(ys.y);
                ldsw<f32x2>(L_Y + sv * 256 + l31 * 8, ys);
                SSTAMP(24);
            } else {
                OP_RUN({ });
                SSTAMP(27);
            }
        }
        lds_flag(W_PDONE + 4 * lw, 1);
        lds_wait<1>(W_PDONE + 4 * (lane & 3));
    } else {
        const int t = wid & 1, hp = wid >> 1, pm = t << 1;
        unsigned xb[4], wl[4], wh[4], w8[4];
        {
            const int row = 32 * t + l31, q = (lane & 15) >> 2, pp = lane & 3, blkc = (lane >> 4) & 1;
            const unsigned wphys = (unsigned)((4 * hp + 2 * blkc + (pp >> 1)) ^ (((q >> 1) & 1) << 2));
#pragma unroll
            for (int c = 0; c < 4; ++c) {
                xb[c] = (row >> 3) * 1024 + (row & 7) * 128 + (((2 * (c ^ pm) + h) ^ ((row >> 1) & 7)) * 16);
                PIN(xb[c]);
            }
#pragma unroll
            for (int c = 0; c < 4; c += 2) {
                wl[c] = (2 * (c ^ pm) + h) * 1024 + q * 128 + wphys * 16 + 8 * (pp & 1);
                wh[c] = wl[c] + 65536;
                PIN(wl[c]); PIN(wh[c]);
                wl[c + 1] = wl[c] + 2048; wh[c + 1] = wh[c] + 2048;
            }
        }
        const unsigned one2 = (lane & 7) == 4 * ((lane >> 4) & 1) ? 0x3f803f80u : 0u;
        bf16x8 csel = asfrag((u32x4){one2, one2, one2, one2});
        unsigned a_full = L_LAND, a_stp = L_STP + (32 * t + l31) * 8;
        unsigned l16 = lane * 16, a_r0 = L_LAND + 4 * (lane & 7);
        PIN(a_r0); PIN(csel); PIN(a_stp); PIN(a_full); PIN(l16);
        unsigned g_k0 = WS_K + (unsigned)(p * 32 + 4 * j + 2 * hp) * 2048u + t * 1024u, g_k1 = g_k0 + 2048;
        asm volatile("" : "+s"(g_k0), "+s"(g_k1));
        const int head = 4 * j + wid;
        unsigned sk3 = WS_K + (unsigned)((p - 1) * 32 + head) * 2048u, sk2 = p >= 2 ? sk3 - 65536 : OOB, sk1 = p >= 3 ? sk3 - 2 * 65536 : OOB, sk0 = p >= 4 ? sk3 - 3 * 65536 : OOB;
        if (p == 0) sk3 = OOB;
        i32x4 sws = mk_srd(ws, WS_USED);
        asm volatile("" : "+s"(sk0), "+s"(sk1), "+s"(sk2), "+s"(sk3), "+s"(sws));
        bf16x8 xf[32], wa[8], wbf[8];
        f32x16 acc = {};
        f32x4 cs4 = {};
        unsigned fullv, fulls, fl;

#define WADDR(pos, s) ((P_SLOT[pos] < 4 ? wl[(s) & 3] : P_SLOT[pos] < 8 ? wh[(s) & 3] : w8[(s) & 3]) + (P_SLOT[pos] & 3) * SLOT + ((s) >> 2) * 8192)
#define RD1(W, pos, s) do { W[s] = ldtr2(WADDR(pos, s), WADDR(pos, s) + 512); SBAR(); } while (0)
#define M1(SW, W, i, s) do { acc = (SW) ? MFMA32(W[s], xf[8 * (i) + (s)], acc) : MFMA32(xf[8 * (i) + (s)], W[s], acc); PIN(acc); SBAR(); } while (0)
#define ABL 0
#define C1(W, s) do { if (!((ABL & 1) && ((s) & 2)) && !(ABL & 4)) { cs4 = MFMA16(csel, W[s], cs4); PIN(cs4); SBAR(); } } while (0)
#define RX2(i, s) do { xf[8 * (i) + (s)] = ldsr<bf16x8>(xb[(s) & 3] + (i) * SLOT + ((s) >> 2) * 8192); xf[8 * (i) + (s) + 1] = ldsr<bf16x8>(xb[((s) + 1) & 3] + (i) * SLOT + ((s) >> 2) * 8192); SBAR(); } while (0)
#define RD_W(W, pos) do { RD1(W, pos, 0); RD1(W, pos, 1); RD1(W, pos, 4); RD1(W, pos, 5); RD1(W, pos, 2); RD1(W, pos, 3); RD1(W, pos, 6); RD1(W, pos, 7); } while (0)
#define PEEK(pos) do { fullv = ldsv<unsigned>(a_full + 4 * (pos)); SBAR(); } while (0)
#define GRAB() do { fulls = rfl(fullv); asm volatile("" : "+s"(fulls)); SBAR(); } while (0)
#define TEST(pos) do { if (__builtin_expect(fulls != 0, 0)) { land_wait<pos>(a_full); if ((pos) == 15) asm volatile(".Lfb_code3:" ::: "memory"); } CFENCE(); SBAR(); } while (0)
#define K_STAGE(i, W, Wn, pw) do { M1(1, W, i, 0); if (ORD && (i) == 1) { land_chk<PX2>(fullv, a_full); SBAR(); } if ((i) == 2) { dma_zero(s0, L_LAND + 128 + 4 * wid); SBAR(); } \
        RX2((i) + 1, 0); C1(W, 0); M1(1, W, i, 1); RX2((i) + 1, 4); C1(W, 1); M1(1, W, i, 4); PEEK(pw); RX2((i) + 1, 2); C1(W, 4); M1(1, W, i, 5); RX2((i) + 1, 6); C1(W, 5); M1(1, W, i, 2); \
        land_chk<pw>(fullv, a_full); SBAR(); STAMP(19 + 2 * (i)); \
        RD1(Wn, pw, 0); C1(W, 2); RD1(Wn, pw, 1); M1(1, W, i, 3); RD1(Wn, pw, 4); C1(W, 3); RD1(Wn, pw, 5); M1(1, W, i, 6); RD1(Wn, pw, 2); C1(W, 6); RD1(Wn, pw, 3); M1(1, W, i, 7); RD1(Wn, pw, 6); C1(W, 7); RD1(Wn, pw, 7); } while (0)

        if (PREW & 4) { TAG(); BSTORE(((u32x4){~tag, ~tag, ~tag, ~tag}), rs_ws, lane < 24 ? l16 : OOB, WS_FK + (blk * 4 + wid) * 512, SC1); }
        STAMP(16 + (1)); land_wait<16>(a_r0); STAMP(16 + (2));
        if (!(PREW & 4)) TAG();
        RX2(0, 0); RD1(wa, 1, 0); RD1(wa, 1, 1); RX2(0, 4); RD1(wa, 1, 4); RD1(wa, 1, 5); RX2(0, 2); RD1(wa, 1, 2); RD1(wa, 1, 3); RX2(0, 6); RD1(wa, 1, 6); RD1(wa, 1, 7);
        PEEK(2);
        M1(1, wa, 0, 0); C1(wa, 0); M1(1, wa, 0, 1); C1(wa, 1); M1(1, wa, 0, 4); C1(wa, 4);
        asm volatile("" ::"v"(fullv));
        dma_zero(s0, L_LAND + 96 + 4 * wid); SBAR();
        M1(1, wa, 0, 5); land_chk<2>(fullv, a_full); SBAR(); PEEK(PK1); RX2(1, 0); C1(wa, 5); M1(1, wa, 0, 2); RX2(1, 4); C1(wa, 2); RX2(1, 2); M1(1, wa, 0, 3); RX2(1, 6); C1(wa, 3);
        land_chk<PK1>(fullv, a_full); SBAR(); STAMP(19);
        RD1(wbf, PK1, 0); RD1(wbf, PK1, 1); M1(1, wa, 0, 6); RD1(wbf, PK1, 4); C1(wa, 6); RD1(wbf, PK1, 5); M1(1, wa, 0, 7); RD1(wbf, PK1, 2); C1(wa, 7); RD1(wbf, PK1, 3); RD1(wbf, PK1, 6); RD1(wbf, PK1, 7);
        if (ORD) PEEK(PX2);
        K_STAGE(1, wbf, wa, 6);
        K_STAGE(2, wa, wbf, 7);
#define PC 1
        w8[0] = wh[0] + 65536; w8[2] = wh[2] + 65536; PIN(w8[0]); PIN(w8[2]); w8[1] = w8[0] + 2048; w8[3] = w8[2] + 2048;
        PEEK(8);
        u32x4 cfr = {}, mfr = {};
        M1(1, wbf, 3, 0); PIN(cfr); SBAR(); C1(wbf, 0); M1(1, wbf, 3, 1); PIN(mfr); SBAR(); C1(wbf, 1); M1(1, wbf, 3, 4); GRAB(); dma_zero(s0, L_LAND + 160 + 4 * wid); SBAR(); C1(wbf, 4);
        M1(1, wbf, 3, 5); TEST(8); STAMP(33); RD1(wa, 8, 0); C1(wbf, 5); RD1(wa, 8, 1); M1(1, wbf, 3, 2); PEEK(9); RD1(wa, 8, 4); C1(wbf, 2); RD1(wa, 8, 5); M1(1, wbf, 3, 3); RD1(wa, 8, 2); C1(wbf, 3);
        RD1(wa, 8, 3); M1(1, wbf, 3, 6); RD1(wa, 8, 6); C1(wbf, 6); RD1(wa, 8, 7); M1(1, wbf, 3, 7); GRAB();
        f32x2 st0 = ldsv<f32x2>(a_stp), st1 = ldsv<f32x2>(a_stp + 512), st2 = ldsv<f32x2>(a_stp + 1024); SBAR();
        C1(wbf, 7);
        __builtin_amdgcn_s_setprio(PC);
        STAMP(25);
        f32x16 av = {};
        f32x4 cv4 = {};
#define MV(W, i, s) do { av = MFMA32(xf[8 * (i) + (s)], W[s], av); PIN(av); SBAR(); } while (0)
#define CV(W, s) do { if (!((ABL & 2) && ((s) & 2)) && !(ABL & 4)) { cv4 = MFMA16(csel, W[s], cv4); PIN(cv4); SBAR(); } } while (0)
#define V_STAGE(i, W, Wn, pos, H0, H1, H2, H3, H4, H5, H6, H7) do { MV(W, i, 0); TEST(pos); RD1(Wn, pos, 0); H0; SBAR(); CV(W, 0); MV(W, i, 1); RD1(Wn, pos, 1); H1; SBAR(); CV(W, 1); \
        MV(W, i, 4); RD1(Wn, pos, 4); H2; SBAR(); CV(W, 4); MV(W, i, 5); RD1(Wn, pos, 5); H3; SBAR(); CV(W, 5); MV(W, i, 2); PEEK((pos) + 1); RD1(Wn, pos, 2); H4; SBAR(); CV(W, 2); \
        MV(W, i, 3); RD1(Wn, pos, 3); H5; SBAR(); CV(W, 3); MV(W, i, 6); RD1(Wn, pos, 6); H6; SBAR(); CV(W, 6); MV(W, i, 7); GRAB(); RD1(Wn, pos, 7); H7; SBAR(); CV(W, 7); } while (0)
#define KFLAG 4
#define PUB_K(n) do { if (KFLAG == (n)) { drain(); PUTFLAG(WS_FK + (blk * 4 + wid) * 512); CFENCE(); if (RPF & 1) touch_ws(sws, l16, g_k0, 0x0000ffff0000ffffull); SBAR(); STAMP(3); } } while (0)
        float cs_, mean, var, mb, rstd;
        u32x4 k0, k1;
        u64 stbad;
#define SMUL 1
#define MUL1(a, b) (SMUL ? mul_np((a), (b)) : (a) * (b))
#define KSC(r) do { acc[r] = MUL1(acc[r], rstd); acc[r + 1] = MUL1(acc[r + 1], rstd); acc[r + 2] = MUL1(acc[r + 2], rstd); acc[r + 3] = MUL1(acc[r + 3], rstd); } while (0)
        V_STAGE(0, wa, wbf, 9,
                { cs_ = LM(l & 32) ? 0.f : cs4[0]; mean = rbf(cs_); PIN(cs_); PIN(mean); },
                { cfr[0] = pk2(mean, cs_ - mean); PIN(cfr); },
                { stbad = __ballot(max(max(__float_as_uint(st0.y), __float_as_uint(st1.y)), __float_as_uint(st2.y)) == ~0u); asm volatile("" : "+s"(stbad)); },
                { if (__builtin_expect(stbad != 0, 0)) st3_wait(st0, st1, st2, a_stp); st0 = ((st0 + st1) + st2) * (1.f / D_); PIN(st0); },
                { var = fmaxf(st0.y - st0.x * st0.x, 0.f); mb = rbf(st0.x); PIN(var); PIN(mb); },
                { mfr[0] = pk2(-mb, -mb); var = rbf(var) + 1e-5f; PIN(mfr); PIN(var); },
                { var = __builtin_amdgcn_sqrtf(rbf(var)); PIN(var); SBAR(); acc = MFMA32(asfrag(cfr), asfrag(mfr), acc); PIN(acc); },
                { rstd = __builtin_amdgcn_rcpf(rbf(var)); PIN(rstd); });
        const bf16x8 mfrag = asfrag(mfr);
        STAMP(34);
        V_STAGE(1, wbf, wa, 10,
                { KSC(0); PIN(acc); }, { KSC(4); PIN(acc); }, { k0 = pack8(acc, 0); PIN(k0); }, { KSC(8); PIN(acc); }, { KSC(12); PIN(acc); }, { k1 = pack8(acc, 8); PIN(k1); },
                { BSTORE2(k0, k1, rs_ws, l16, g_k0, g_k1, SC1); STAMP(2); }, { });
        STAMP(32);
        f32x4 rs4[4];
        unsigned a_rs = L_RSTD + (32 * t + 4 * h) * 4, lq = l16, vo;
        V_STAGE(2, wa, wbf, 11, { fl = ldsv<unsigned>(W_STAT); PIN(a_rs); }, { }, { }, { fl = rfl(fl); asm volatile("" : "+s"(fl)); },
                { if (__builtin_expect(fl == 0, 0)) lds_wait<1>(W_STAT); CFENCE(); }, { }, { }, { });
        STAMP(35);
        MV(wbf, 3, 0); TEST(12); RD1(wa, 12, 0); CV(wbf, 0); CV(wbf, 2); MV(wbf, 3, 1); RD1(wa, 12, 1); CV(wbf, 1); CV(wbf, 3); PEEK(13);
        MV(wbf, 3, 4); RD1(wa, 12, 4); CV(wbf, 4); CV(wbf, 6); MV(wbf, 3, 5); RD1(wa, 12, 5); CV(wbf, 5); CV(wbf, 7); RD1(wa, 12, 2);
        MV(wbf, 3, 2); RD1(wa, 12, 3); GRAB();
        MV(wbf, 3, 3); cs_ = LM(l & 32) ? 0.f : cv4[0]; mean = rbf(cs_); cfr[0] = pk2(mean, cs_ - mean); PIN(cfr); SBAR();
        MV(wbf, 3, 6); rs4[0] = ldsr<f32x4>(a_rs); rs4[1] = ldsr<f32x4>(a_rs + 32); SBAR(); RD1(wa, 12, 6);
        MV(wbf, 3, 7); rs4[2] = ldsr<f32x4>(a_rs + 64); rs4[3] = ldsr<f32x4>(a_rs + 96); SBAR(); RD1(wa, 12, 7);
        av = MFMA32(mfrag, asfrag(cfr), av); PIN(av); SBAR(); PUB_K(4);
        STAMP(36);
        acc = (f32x16){}; cs4 = (f32x4){};
        unsigned a_csw, a_csr;
        u32x4 v0, v1;
#define VSC(r) do { av[r] = MUL1(av[r], rs4[(r) >> 2][0]); av[r + 1] = MUL1(av[r + 1], rs4[(r) >> 2][1]); av[r + 2] = MUL1(av[r + 2], rs4[(r) >> 2][2]); av[r + 3] = MUL1(av[r + 3], rs4[(r) >> 2][3]); } while (0)
#define Q_STAGE(i, W, Wn, pos, H0, H1, H2, H3, H4, H5, H6, H7) do { M1(1, W, i, 0); TEST(pos); RD1(Wn, pos, 0); H0; SBAR(); M1(1, W, i, 1); RD1(Wn, pos, 1); H1; SBAR(); \
        M1(1, W, i, 4); RD1(Wn, pos, 4); H2; SBAR(); M1(1, W, i, 5); RD1(Wn, pos, 5); H3; SBAR(); M1(1, W, i, 2); if ((pos) < 15) PEEK((pos) + 1); RD1(Wn, pos, 2); H4; SBAR(); C1(Wn, 0); \
        M1(1, W, i, 3); RD1(Wn, pos, 3); H5; SBAR(); C1(Wn, 1); M1(1, W, i, 6); RD1(Wn, pos, 6); H6; SBAR(); C1(Wn, 4); M1(1, W, i, 7); if ((pos) < 15) GRAB(); RD1(Wn, pos, 7); H7; SBAR(); C1(Wn, 5); } while (0)
#define VDIR 0
        Q_STAGE(0, wa, wbf, 13,
                { C1(wa, 0); PIN(lq); vo = ((lq >> 8) & 1) * 2048 + (lq >> 9) * 256 + (lq & 240); PIN(vo); SBAR(); C1(wa, 1); }, { VSC(0); VSC(4); PIN(av); SBAR(); C1(wa, 4); v0 = pack8(av, 0); PIN(v0); }, { VSC(8); VSC(12); PIN(av); SBAR(); C1(wa, 5); v1 = pack8(av, 8); PIN(v1); },
                { if (VDIR) BSTORE2(v0, v1, rs_ws, vo, g_k0 + (WS_V - WS_K), g_k0 + (WS_V - WS_K) + 512, SC1); else { vo += L_VOWN + (4 * hp + t) * 1024; ldsw<u32x4>(vo, v0); ldsw<u32x4>(vo + 512, v1); } },
                { if (!VDIR) lds_flag(W_VOWN + 4 * wid, 1); STAMP(4); },
                { if (VDIR) { vo += L_VOWN + (4 * hp + t) * 1024; ldsw<u32x4>(vo, v0); ldsw<u32x4>(vo + 512, v1); } }, { if (VDIR) lds_flag(W_VOWN + 4 * wid, 1); }, { });
        STAMP(42);
        unsigned therm, va, vown;
        Q_STAGE(1, wbf, wa, 14,
                { PUB_K(5); }, { }, { }, { }, { a_csw = L_CSP + wid * 128 + ((lq >> 2) & 124); PIN(a_csw); }, { a_csr = L_CSP + hp * 256 + (lq >> 2); PIN(a_csr); },
                { therm = (unsigned)((int)(~1u << ((lq >> 4) & 31)) >> ((lq >> 7) & 4)); PIN(therm); },
                { vown = ((lq >> 1) & 256) + (lq & 240); PIN(vown); });
        STAMP(43);
        Q_STAGE(2, wa, wbf, 15,
                { }, { }, { }, { if (VDIR == 1) { drain(); lds_flag(W_VOWN + 4 * wid, 2); } }, { ldsw<u32x4>(L_KOWN + (4 * hp + t) * 1024 + l16, k0); }, { ldsw<u32x4>(L_KOWN + (4 * hp + 2 + t) * 1024 + l16, k1); },
                { va = (LM(l & 16) ? L_ONES8 : L_VREM + wid * VREM_STRIDE) + vown; PIN(va); }, { vown += LM(l & 16) ? L_ONES : L_VOWN + wid * 2048; PIN(vown); });
        STAMP(39);
        bf16x8 kf[8];
#define KLOOK(v) asm volatile("s_cmp_lg_u32 %8, 0\n\ts_cbranch_scc1 1f\n\tv_readfirstlane_b32 %8, %9\n\ts_cmp_eq_u32 %8, 0\n\ts_cbranch_scc1 1f\n\t" \
        "buffer_load_dwordx4 %0, %10, %11, %12 offen sc1\n\tbuffer_load_dwordx4 %1, %10, %11, %12 offen offset:1024 sc1\n\tbuffer_load_dwordx4 %2, %10, %11, %13 offen sc1\n\t" \
        "buffer_load_dwordx4 %3, %10, %11, %13 offen offset:1024 sc1\n\tbuffer_load_dwordx4 %4, %10, %11, %14 offen sc1\n\tbuffer_load_dwordx4 %5, %10, %11, %14 offen offset:1024 sc1\n\t" \
        "buffer_load_dwordx4 %6, %10, %11, %15 offen sc1\n\tbuffer_load_dwordx4 %7, %10, %11, %15 offen offset:1024 sc1\n1:" \
        : "+v"(kf[0]), "+v"(kf[1]), "+v"(kf[2]), "+v"(kf[3]), "+v"(kf[4]), "+v"(kf[5]), "+v"(kf[6]), "+v"(kf[7]), "+s"(kin) \
        : "v"(v), "v"(l16), "s"(sws), "s"(sk0), "s"(sk1), "s"(sk2), "s"(sk3) : "scc", "memory")
#define KWAIT(i) asm volatile("s_waitcnt vmcnt(%1)" : "+v"(kf[i]) : "i"(7 - (i)) : "memory")
        unsigned a_w = L_WORDS, a_kq = L_KOWN + wid * 2048 + l16, kin = 0;
        f32x16 cm, cf;
#define CM2(r) do { asm volatile("v_bfe_i32 %0, %2, %3, 1\n\tv_bfe_i32 %1, %2, %3+1, 1\n\tv_lshlrev_b32 %0, 23, %0\n\tv_lshlrev_b32 %1, 23, %1" \
        : "=&v"(cm[r]), "=&v"(cm[r + 1]) : "v"(therm), "i"(((r) & 3) + 2 * ((r) & 12))); SBAR(); } while (0)
        M1(1, wbf, 3, 0); PIN(a_w); PIN(a_kq); rstd *= 0.25f * 1.4426950408889634f; PIN(rstd); SBAR(); ldsw<float>(a_csw, cs4[0]); SBAR();
        if (VDIR == 2) { drain(); lds_flag(W_VOWN + 4 * wid, 2); SBAR(); }
        M1(1, wbf, 3, 1); CM2(0);
        M1(1, wbf, 3, 4); CM2(2);
        M1(1, wbf, 3, 5); cs_ = ldsv<float>(a_csr); fullv = ldsv<unsigned>(a_w + (W_KSEEN - L_WORDS)); lds_flag(W_FREE + 4 * wid, 1); SBAR();
        M1(1, wbf, 3, 2); CM2(4);
        M1(1, wbf, 3, 3); CM2(6);
        M1(1, wbf, 3, 6);
        val_chk<0>(cs_, a_csr); mean = rbf(cs_); PIN(mean); SBAR();
        M1(1, wbf, 3, 7);
        cfr[0] = pk2(mean, cs_ - mean); PIN(cfr); SBAR();
        const bf16x8 k8 = ldsr<bf16x8>(a_kq), k9 = ldsr<bf16x8>(a_kq + 1024); SBAR();
        acc = MFMA32(asfrag(cfr), mfrag, acc); PIN(acc); SBAR();
        asm volatile("" : "=v"(kf[0]), "=v"(kf[1]), "=v"(kf[2]), "=v"(kf[3]), "=v"(kf[4]), "=v"(kf[5]), "=v"(kf[6]), "=v"(kf[7]));
        KLOOK(fullv); SBAR();
#define CF8(r0) do { _Pragma("unroll") for (int r = (r0); r < (r0) + 8; ++r) cf[r] = __uint_as_float(__float_as_uint(cm[r]) ^ 0xff800000u); \
        asm volatile("" : "+v"(cf[r0]), "+v"(cf[r0 + 1]), "+v"(cf[r0 + 2]), "+v"(cf[r0 + 3]), "+v"(cf[r0 + 4]), "+v"(cf[r0 + 5]), "+v"(cf[r0 + 6]), "+v"(cf[r0 + 7])); SBAR(); } while (0)
        CF8(0);
        STAMP(40);
        acc *= rstd;
        ldsw<u32x4>(L_QOWN + (4 * hp + t) * 1024 + l16, pack8(acc, 0)); ldsw<u32x4>(L_QOWN + (4 * hp + 2 + t) * 1024 + l16, pack8(acc, 8));
        lds_flag(W_PAIR + 4 * wid, 1); SBAR();
        STAMP(41);
        const bf16x8 v8a = ldsr<bf16x8>(vown), v8b = ldsr<bf16x8>(vown + 512), v9a = ldsr<bf16x8>(vown + 1024), v9b = ldsr<bf16x8>(vown + 1536);
        SBAR(); CM2(8); CM2(10); CM2(12); CM2(14);
        bf16x8 q0, q1;
        {
            unsigned v, c;
            asm volatile("s_mov_b32 %1, %7\n1:\n\tds_read_b32 %0, %5\n\tds_read_b128 %2, %6 offset:8192\n\tds_read_b128 %3, %6 offset:9216\n\tds_read_b32 %4, %8 offset:%9\n\ts_waitcnt lgkmcnt(0)\n\t"
                         "v_cmp_eq_u32 vcc, 0, %0\n\ts_cbranch_vccz 2f\n\ts_sub_u32 %1, %1, 1\n\ts_cbranch_scc0 1b\n2:"
                         : "=&v"(v), "=&s"(c), "=&v"(q0), "=&v"(q1), "=&v"(fullv) : "v"(W_PAIR + 4 * (wid ^ 1)), "v"(a_kq), "i"(SPIN_LDS), "v"(a_w), "i"(W_KSEEN - L_WORDS) : "vcc", "scc", "memory");
        }
        STAMP(5);
        f32x16 o0, o1;
        const f32x16 z16 = {};
#define EXPP(Sx, Pa, Pb) do { _Pragma("unroll") for (int r = 0; r < 16; ++r) Sx[r] = __builtin_amdgcn_exp2f(Sx[r]); Pa = asfrag(pack8(Sx, 0)); Pb = asfrag(pack8(Sx, 8)); } while (0)
#define EX4(E, Sx, r) do { E[r] = __builtin_amdgcn_exp2f(Sx[r]); E[r + 1] = __builtin_amdgcn_exp2f(Sx[r + 1]); E[r + 2] = __builtin_amdgcn_exp2f(Sx[r + 2]); E[r + 3] = __builtin_amdgcn_exp2f(Sx[r + 3]); \
        asm volatile("" : "+v"(E[r]), "+v"(E[r + 1]), "+v"(E[r + 2]), "+v"(E[r + 3])); SBAR(); } while (0)
#define PK(E, Pa, Pb) do { Pa = asfrag((u32x4){pk2(E[0], E[1]), pk2(E[2], E[3]), pk2(E[4], E[5]), pk2(E[6], E[7])}); \
        Pb = asfrag((u32x4){pk2(E[8], E[9]), pk2(E[10], E[11]), pk2(E[12], E[13]), pk2(E[14], E[15])}); asm volatile("" : "+v"(Pa), "+v"(Pb)); SBAR(); } while (0)
        float e[16];
        u32x4 of0, of1;
        float l0, l1;
        const unsigned g_o = WS_O + (unsigned)(p * 64 + head) * 1024u;
#define FIN(O, L, F) do { L = O[8]; const float i_ = __builtin_amdgcn_rcpf(L); F = (u32x4){pk2(O[0] * i_, O[1] * i_), pk2(O[2] * i_, O[3] * i_), pk2(O[4] * i_, O[5] * i_), pk2(O[6] * i_, O[7] * i_)}; } while (0)
        {
            bf16x8 P[2][8][2];
            f32x16 sA, sB;
            bf16x8 pa, pb, pc, pd;
            sA = MFMA32(k8, q0, cm); PIN(sA); SBAR();
            sB = MFMA32(k8, q1, z16); PIN(sB); SBAR();
            KLOOK(fullv); fullv = ldsv<unsigned>(a_w + (W_KSEEN - L_WORDS)); SBAR();
            CF8(8);
            EX4(e, sA, 0); EX4(e, sA, 4); EX4(e, sA, 8); EX4(e, sA, 12); PK(e, pa, pb);
            sA = MFMA32(k9, q1, cm); PIN(sA); SBAR();
            KLOOK(fullv); SBAR();
            EX4(e, sB, 0); o0 = MFMA32(v8a, pa, z16); PIN(o0); SBAR(); EX4(e, sB, 4); o0 = MFMA32(v8b, pb, o0); PIN(o0); SBAR(); EX4(e, sB, 8); EX4(e, sB, 12); PK(e, pc, pd);
            if (__builtin_expect(!kin, 0)) lds_wait<1>(a_w + (W_KSEEN - L_WORDS));
            KLOOK(a_w); SBAR();
            o1 = MFMA32(v8a, pc, z16); PIN(o1); SBAR(); EX4(e, sA, 0); o1 = MFMA32(v8b, pd, o1); PIN(o1); SBAR(); EX4(e, sA, 4); EX4(e, sA, 8); EX4(e, sA, 12);
            STAMP(6);
            KWAIT(0); sA = MFMA32(kf[0], q0, cf); PIN(sA); SBAR();
            PK(e, pa, pb);
#define UA(q, i, Sc, Sn, NEXT) do { Sn = NEXT; PIN(Sn); SBAR(); EX4(e, Sc, 0); EX4(e, Sc, 4); EX4(e, Sc, 8); EX4(e, Sc, 12); PK(e, P[q][i][0], P[q][i][1]); } while (0)
            KWAIT(1); sB = MFMA32(kf[1], q1, cf); PIN(sB); SBAR();
            EX4(e, sA, 0); o1 = MFMA32(v9a, pa, o1); PIN(o1); SBAR(); EX4(e, sA, 4); o1 = MFMA32(v9b, pb, o1); PIN(o1); SBAR(); EX4(e, sA, 8); EX4(e, sA, 12); PK(e, P[0][0][0], P[0][0][1]);
            STAMP(44);
            UA(1, 1, sB, sA, MFMA32(kf[1], q0, z16));
            KWAIT(2); UA(0, 1, sA, sB, MFMA32(kf[2], q0, z16));
            UA(0, 2, sB, sA, MFMA32(kf[2], q1, z16));
            KWAIT(3); UA(1, 2, sA, sB, MFMA32(kf[3], q0, z16));
            KWAIT(4); UA(0, 3, sB, sA, MFMA32(kf[4], q0, z16));
            KWAIT(5); UA(0, 4, sA, sB, MFMA32(kf[5], q0, z16));
            KWAIT(6); UA(0, 5, sB, sA, MFMA32(kf[6], q0, z16));
            KWAIT(7); UA(0, 6, sA, sB, MFMA32(kf[7], q0, z16));
            sA = MFMA32(kf[3], q1, z16); PIN(sA); SBAR(); EX4(e, sB, 0);
            fullv = ldsv<unsigned>(W_VRDY + 4 * hp); SBAR();
            EX4(e, sB, 4); EX4(e, sB, 8); GRAB(); EX4(e, sB, 12);
            STAMP(8);
            if (__builtin_expect(fulls == 0, 0)) lds_wait<1>(W_VRDY + 4 * hp);
            CFENCE(); SBAR();
            STAMP(9);
            bf16x8 Vf[30];
#define VR(n) do { if ((n) < 30) { Vf[(n) % 30] = ldsr<bf16x8>(va + ((n) < 16 ? (n) : (n) - 14) * 512); SBAR(); } } while (0)
            VR(0); VR(1); VR(2); VR(3);
            PK(e, P[0][7][0], P[0][7][1]);
#define PV(n) do { if ((n) < 16) { o0 = MFMA32(Vf[n], P[0][((n) >> 1) & 7][(n) & 1], o0); PIN(o0); } else { o1 = MFMA32(Vf[n], P[1][(((n) >> 1) - 7) & 7][(n) & 1], o1); PIN(o1); } SBAR(); VR((n) + 4); } while (0)
#define EX3(Sx, r) do { e[r] = __builtin_amdgcn_exp2f(Sx[r]); e[r + 1] = __builtin_amdgcn_exp2f(Sx[r + 1]); e[r + 2] = __builtin_amdgcn_exp2f(Sx[r + 2]); \
            asm volatile("" : "+v"(e[r]), "+v"(e[r + 1]), "+v"(e[r + 2])); SBAR(); } while (0)
#define PKH(D, r) do { D = asfrag((u32x4){pk2(e[r], e[r + 1]), pk2(e[r + 2], e[r + 3]), pk2(e[r + 4], e[r + 5]), pk2(e[r + 6], e[r + 7])}); PIN(D); SBAR(); } while (0)
#define UB(u, Sc, Sn) do { Sn = MFMA32(kf[(u) + 4], q1, z16); PIN(Sn); SBAR(); EX3(Sc, 0); PV(6 * (u)); EX3(Sc, 3); PV(6 * (u) + 1); EX3(Sc, 6); PV(6 * (u) + 2); EX3(Sc, 9); PV(6 * (u) + 3); \
            EX3(Sc, 12); PV(6 * (u) + 4); e[15] = __builtin_amdgcn_exp2f(Sc[15]); PIN(e[15]); SBAR(); PKH(P[1][(u) + 3][0], 0); PV(6 * (u) + 5); PKH(P[1][(u) + 3][1], 8); } while (0)
            UB(0, sA, sB); UB(1, sB, sA); UB(2, sA, sB);
            FIN(o0, l0, of0); BSTORE(of0, rs_ws, l16, g_o, SC1); SBAR();
            UB(3, sB, sA);
            PV(24); EX4(e, sA, 0); PV(25); EX4(e, sA, 4); PV(26); PKH(P[1][7][0], 0); PV(27); EX4(e, sA, 8); PV(28); EX4(e, sA, 12); PKH(P[1][7][1], 8); PV(29);
            FIN(o1, l1, of1);
        }
        const bool safe = max(__float_as_uint(l0) - 0x21800000u, __float_as_uint(l1) - 0x21800000u) <= 0x5d800000u - 0x21800000u;
#define FORCE_FB 0
        if (__builtin_expect(!__all(safe) || (FORCE_FB && ((blk + wid) % FORCE_FB) == 0), 0)) {
            poll_flags(ws, (lane & 2) ? WS_FV + blk * 512 : WS_FK + (blk * 4 + 2 * hp + (lane & 1)) * 512, tag);
#pragma unroll 1
            for (int qt = 0; qt < 2; ++qt) {
                const bf16x8 qq = qt ? q1 : q0;
                float m = -INFINITY;
                f32x16 oo = {};
#pragma unroll 1
                for (int pass = 0; pass < 2; ++pass) {
#pragma unroll 1
                    for (int i = 0; i <= 8; ++i) {
                        const int kt = 2 * p + qt - 8 + i;
                        if (kt < 0) continue;
                        const unsigned go = (unsigned)((kt >> 1) * 32 + head) * 2048u + (kt & 1) * 1024u;
                        f32x16 s = MFMA32(asfrag(BLOAD(rs_ws, lane * 16, WS_K + go, SC1)), qq, (f32x16){});
#pragma unroll
                        for (int r = 0; r < 16; ++r) { const int dk = l31 + 32 * (8 - i) - ((r & 3) + 8 * (r >> 2) + 4 * h); if (dk < 0 || dk >= 256) s[r] = -INFINITY; }
                        if (pass == 0) {
#pragma unroll
                            for (int r = 0; r < 16; ++r) m = fmaxf(m, s[r]);
                        } else {
                            const unsigned vlane = h * 256 + (lane & 15) * 16;
                            u32x4 va_ = BLOAD(rs_ws, vlane, WS_V + go, SC1), vb_ = BLOAD(rs_ws, vlane, WS_V + go + 512, SC1);
                            if (l31 >= 16) va_ = vb_ = (u32x4){0x3f803f80u, 0x3f803f80u, 0x3f803f80u, 0x3f803f80u};
#pragma unroll
                            for (int r = 0; r < 16; ++r) s[r] -= m;
                            bf16x8 pa, pb; EXPP(s, pa, pb);
                            oo = MFMA32(asfrag(va_), pa, oo); oo = MFMA32(asfrag(vb_), pb, oo);
                        }
                    }
                    if (pass == 0) { auto r = __builtin_amdgcn_permlane32_swap(__float_as_uint(m), __float_as_uint(m), false, false); m = fmaxf(__uint_as_float(r[0]), __uint_as_float(r[1])); }
                }
                u32x4 ff; FIN(oo, l0, ff);
                if (qt) of1 = ff; else BSTORE(ff, rs_ws, l16, g_o, SC1);
            }
        }
        BSTORE(of1, rs_ws, l16, g_o + 32768, SC1);
        STAMP(10);
        wait_vm<1>();
        PUTFLAG(WS_FK + (blk * 4 + wid) * 512 + 128); CFENCE();
        if (RPF & 4) touch_ws(sws, l16, g_o, 0xffffull);
        lds_flag(W_ODRN + 4 * wid, 1);
        unsigned a_pd;
        {
            OP_PREP(1, wid, L_VREM + wid * VREM_STRIDE);
            wait_vm<(RPF & 4) ? 2 : 1>();
            PUTFLAG(WS_FK + (blk * 4 + wid) * 512 + 256); CFENCE();
            if (RPF & 4) touch_ws(sws, l16, g_o + 32768, 0xffffull);
            STAMP(11);
            xchunk = BLOAD(rs_x, (ln >> 3) * 1024 + (ln & 7) * 16, obase + 32768, 0);
            OP_WO();
            EPI_PREP();
            {
                lds_wait<1>(W_KEYV);
                const bool own_ = ((2047 - (int)(rfl(ldsv<unsigned>(W_KEY)) & 0xffffu)) >> 6) == p;
                a_pd = W_PDONE + 8 + 4 * (own_ ? min(ln & 7, 5) : 2 + (ln & 3));
                PIN(a_pd);
            }
            __builtin_amdgcn_s_sleep(ODLY);
            poll_flags<OSLP>(ws, fo2, tag);
            STAMP(12);
            OP_RUN({ });
        }
        STAMP(13);
        lds_flag(W_PDONE + 16 + 4 * wid, 1);
        lds_wait<1>(a_pd);
    }
    STAMP(14);
    {
        const unsigned key = ldsr<unsigned>(W_KEY);
        const f32x4 a0 = ldsr<f32x4>(e_pa) + ldsr<f32x4>(e_pa + VREM_STRIDE), b0 = ldsr<f32x4>(e_pb) + ldsr<f32x4>(e_pb + VREM_STRIDE);
        const f32x4 a1 = ldsr<f32x4>(e_pa + 16) + ldsr<f32x4>(e_pa + VREM_STRIDE + 16), b1 = ldsr<f32x4>(e_pb + 16) + ldsr<f32x4>(e_pb + VREM_STRIDE + 16);
        if (__builtin_expect((key & 0xffffu) == e_row, 0)) {
            const unsigned u = key >> 16, bits = u ^ ((u & 0x8000u) ? 0x8000u : 0xffffu), yo = L_Y + (tid & 7) * 32;
            const float rs = rbf(__builtin_amdgcn_rcpf(1.f + __expf(-blo(bits))));
            const f32x4 ya = ldsr<f32x4>(yo) + ldsr<f32x4>(yo + 256), yb = ldsr<f32x4>(yo + 16) + ldsr<f32x4>(yo + 272);
#pragma unroll
            for (int e = 0; e < 4; ++e) { za[e] += rbf(rbf(ya[e]) * rs); zb[e] += rbf(rbf(yb[e]) * rs); }
        }
        za += a0 + b0; zb += a1 + b1;
        const u32x4 ov = {pk2(za[0], za[1]), pk2(za[2], za[3]), pk2(zb[0], zb[1]), pk2(zb[2], zb[3])};
#define OUTAUX 0
        __builtin_amdgcn_raw_buffer_store_b128(ov, rs_out, e_oo, obase, OUTAUX);
    }
}

extern "C" void kernel_launch(void* const* d_in, const int* in_sizes, int n_in, void* d_out, int out_size, void* d_ws, size_t ws_size,
                              hipStream_t stream) {
#define WSPOS 0
    const size_t wsoff = DIAG ? 0 : (((ws_size - WS_USED) / 8 * WSPOS) & ~(size_t)((2u << 20) - 1));
    (void)hipFuncSetAttribute((const void*)fused_block, hipFuncAttributeMaxDynamicSharedMemorySize, 163840);
    fused_block<<<256, 512, 163840, stream>>>((const u16*)d_in[0], (const u16*)d_in[1], (const u16*)d_in[4], (char*)d_ws + wsoff, (const u16*)d_in[2], (const u16*)d_in[5],
                                              (const u16*)d_in[6], (const u16*)d_in[7], (u16*)d_out);
}
```
